```python
import torch

def _softmax(x, axis=-1):
    return torch.softmax(x, dim=axis)
def _gelu(x):
    return torch.nn.functional.gelu(x, approximate="tanh")
def _silu(x):
    return torch.nn.functional.silu(x)
def _sigmoid(x):
    return torch.sigmoid(x)
def _layernorm(x, axis=-1, eps=1e-5):
    m = x.mean(dim=axis, keepdim=True)
    v = x.var(dim=axis, keepdim=True, unbiased=False)
    return (x - m) / torch.sqrt(v + eps)
def _scatter_add(tgt, idx, src, axis=0):
    return tgt.clone().index_add_(axis, idx, src)
def _segment_sum(src, seg, n):
    out = torch.zeros((n,) + src.shape[1:], dtype=src.dtype)
    return out.index_add_(0, seg, src)
def _pad(x, pw):
    flat = tuple(v for pair in reversed(pw) for v in pair)
    return torch.nn.functional.pad(x, flat)

kernel_name = 'standalone__gqa_block_cached__near_ridge'

def setup_inputs():

    _w = (lambda a: (lambda t: t.to(torch.bfloat16) if t.is_floating_point() else t)(torch.as_tensor(a)))
    B, Sq, D = 8, 192, 512
    Hq, Hkv, Sk, Dh = 32, 32, 1024, 128
    assert Hq % Hkv == 0, (Hq, Hkv)
    return {
        "x": _w((lambda *s: torch.randn(*s, dtype=torch.bfloat16) * 0.8)(B, Sq, D)),
        "k_cache": _w((lambda *s: torch.randn(*s, dtype=torch.bfloat16) * 0.8)(B, Hkv, Sk, Dh)),
        "v_cache": _w((lambda *s: torch.randn(*s, dtype=torch.bfloat16) * 0.05)(B, Hkv, Sk, Dh)),
        "wq": _w((lambda *s: torch.randn(*s, dtype=torch.bfloat16) * 0.8)(D, Hq * Dh) * (D ** -0.5)),
        "wo": _w((lambda *s: torch.randn(*s, dtype=torch.bfloat16) * 0.05)(Hq * Dh, D) * ((Hq * Dh) ** -0.5)),
    }

def reference(x, k_cache, v_cache, wq, wo):
    B, Sq, D = x.shape
    Hq, Dh = 32, 128
    Hkv = k_cache.shape[1]
    rep = Hq // Hkv
    q = (lambda x, *d: x.permute(*d))((x @ wq).reshape(B, Sq, Hq, Dh), 0, 2, 1, 3)
    k = (lambda x, n, axis: torch.repeat_interleave(x, n, dim=axis))(k_cache, rep, axis=1)
    v = (lambda x, n, axis: torch.repeat_interleave(x, n, dim=axis))(v_cache, rep, axis=1)
    scores = torch.einsum("bhqd,bhkd->bhqk", q, k) * (Dh ** -0.5)
    p = _softmax(scores, axis=-1)
    o = torch.einsum("bhqk,bhkd->bhqd", p, v)
    return (lambda x, *d: x.permute(*d))(o, 0, 2, 1, 3).reshape(B, Sq, Hq * Dh) @ wo
```

```cpp
#include <hip/hip_runtime.h>
#include <hip/hip_bf16.h>
typedef float    f32x16 __attribute__((ext_vector_type(16)));
typedef __bf16   bf16x8 __attribute__((ext_vector_type(8)));
typedef unsigned short u16;
__device__ __forceinline__ float b2f(u16 x){ return __uint_as_float(((unsigned)x)<<16); }
__device__ __forceinline__ u16 f2b(float f){ __bf16 b=(__bf16)f; return __builtin_bit_cast(u16,b); }

__global__ __launch_bounds__(256) void gemm_nn(const u16* __restrict__ A, const u16* __restrict__ B, u16* __restrict__ C, int M, int N, int K){
  __shared__ u16 As[64][40];
  __shared__ u16 Bs[64][40];
  int tid=threadIdx.x, lane=tid&63, w=tid>>6;
  int m0=blockIdx.y*64, n0=blockIdx.x*64;
  int wm=(w>>1)*32, wn=(w&1)*32;
  f32x16 acc={0};
  for(int k0=0;k0<K;k0+=32){
    { int r=tid>>2, c=(tid&3)*8; *(uint4*)&As[r][c] = *(const uint4*)&A[(size_t)(m0+r)*K+k0+c]; }
    { int r=tid>>3, c=(tid&7)*8; uint4 v=*(const uint4*)&B[(size_t)(k0+r)*N+n0+c]; u16* p=(u16*)&v; for(int j=0;j<8;j++) Bs[c+j][r]=p[j]; }
    __syncthreads();
    for(int s=0;s<2;s++){
      bf16x8 a=*(bf16x8*)&As[wm+(lane&31)][s*16+(lane>>5)*8];
      bf16x8 b=*(bf16x8*)&Bs[wn+(lane&31)][s*16+(lane>>5)*8];
      acc=__builtin_amdgcn_mfma_f32_32x32x16_bf16(a,b,acc,0,0,0);
    }
    __syncthreads();
  }
  for(int r=0;r<16;r++){
    int row=(r&3)+8*(r>>2)+4*(lane>>5);
    C[(size_t)(m0+wm+row)*N+n0+wn+(lane&31)]=f2b(acc[r]);
  }
}
__global__ __launch_bounds__(64) void attn(const u16* __restrict__ Q, const u16* __restrict__ Kc, const u16* __restrict__ Vc, u16* __restrict__ O){
  __shared__ u16 Vt[128][40];
  int lane=threadIdx.x, r=lane&31, hh=lane>>5;
  int qb=blockIdx.x%6, bh=blockIdx.x/6, b=bh>>5, h=bh&31;
  const u16* Qp=Q+((size_t)(b*192+qb*32+r))*4096+h*128;
  const u16* Kp=Kc+(size_t)bh*1024*128; const u16* Vp=Vc+(size_t)bh*1024*128;
  bf16x8 qf[8];
  for(int s=0;s<8;s++) qf[s]=*(const bf16x8*)&Qp[s*16+hh*8];
  f32x16 o[4]; for(int i=0;i<4;i++) o[i]=(f32x16){0};
  float m=-1e30f,l=0.f; const float c=0.12751743082f;
  for(int k0=0;k0<1024;k0+=32){
    f32x16 s={0};
    for(int t=0;t<8;t++){ bf16x8 kf=*(const bf16x8*)&Kp[(size_t)(k0+r)*128+t*16+hh*8]; s=__builtin_amdgcn_mfma_f32_32x32x16_bf16(kf,qf[t],s,0,0,0);}
    for(int i=0;i<8;i++){ int idx=lane+i*64; int key=idx>>4, dc=(idx&15)*8; uint4 v=*(const uint4*)&Vp[(size_t)(k0+key)*128+dc]; u16* p=(u16*)&v; for(int j=0;j<8;j++) Vt[dc+j][key]=p[j]; }
    float mx=-1e30f; for(int i=0;i<16;i++) mx=fmaxf(mx,s[i]);
    mx=fmaxf(mx,__shfl_xor(mx,32));
    float mn=fmaxf(m,mx*c); float al=exp2f(m-mn); m=mn;
    float rs=0; float p[16]; for(int i=0;i<16;i++){p[i]=exp2f(s[i]*c-mn); rs+=p[i];}
    rs+=__shfl_xor(rs,32); l=l*al+rs;
    for(int i=0;i<4;i++) for(int j=0;j<16;j++) o[i][j]*=al;
    __syncthreads();
    for(int ks=0;ks<2;ks++){
      bf16x8 pf; for(int j=0;j<8;j++) pf[j]=(__bf16)p[8*ks+j];
      for(int db=0;db<4;db++){
        bf16x8 vf; for(int j=0;j<8;j++){ int key=16*ks+8*(j>>2)+4*hh+(j&3); vf[j]=__builtin_bit_cast(__bf16,Vt[db*32+r][key]); }
        o[db]=__builtin_amdgcn_mfma_f32_32x32x16_bf16(vf,pf,o[db],0,0,0);
      }
    }
    __syncthreads();
  }
  float il=1.f/l;
  u16* Op=O+((size_t)(b*192+qb*32+r))*4096+h*128;
  for(int db=0;db<4;db++) for(int i=0;i<16;i++){ int d=db*32+(i&3)+8*(i>>2)+4*hh; Op[d]=f2b(o[db][i]*il);}
}
extern "C" void kernel_launch(void* const* d_in, const int* in_sizes, int n_in, void* d_out, int out_size, void* d_ws, size_t ws_size, hipStream_t stream){
  const u16* x=(const u16*)d_in[0]; const u16* k=(const u16*)d_in[1]; const u16* v=(const u16*)d_in[2]; const u16* wq=(const u16*)d_in[3]; const u16* wo=(const u16*)d_in[4];
  u16* q=(u16*)d_ws; u16* o=q+(size_t)1536*4096;
  gemm_nn<<<dim3(64,24),256,0,stream>>>(x,wq,q,1536,4096,512);
  attn<<<256*6,64,0,stream>>>(q,k,v,o);
  gemm_nn<<<dim3(8,24),256,0,stream>>>(o,wo,(u16*)d_out,1536,512,4096);
}
```

```cpp
#include <hip/hip_runtime.h>
#include <hip/hip_bf16.h>
typedef unsigned short u16;
typedef unsigned u32x4 __attribute__((ext_vector_type(4)));
#define GL __attribute__((address_space(1)))
typedef const unsigned long long __attribute__((address_space(4)))* args_t;

__device__ __forceinline__ float blo(unsigned x) { return __uint_as_float(x << 16); }
__device__ __forceinline__ float bhi(unsigned x) { return __uint_as_float(x & 0xffff0000u); }
__device__ __forceinline__ unsigned f2b(float f) { __bf16 b = (__bf16)f; return __builtin_bit_cast(u16, b); }
__device__ __forceinline__ float rl(float x, int l) { return __uint_as_float(__builtin_amdgcn_readlane(__float_as_uint(x), l)); }
__device__ __forceinline__ float bperm(float x, int l) { return __uint_as_float(__builtin_amdgcn_ds_bpermute(l << 2, __float_as_uint(x))); }

__global__ __launch_bounds__(512) __attribute__((amdgpu_waves_per_eu(2, 2)))
void fused(const u16* x_, const u16* k_, const u16* v_, const u16* wq_, const u16* wo_, u16* out_, char* ws_)
{
  __shared__ char lds[163840];
  unsigned g = blockIdx.x;
  unsigned wid = __builtin_amdgcn_readfirstlane(threadIdx.x >> 6);
  args_t ka = (args_t)__builtin_amdgcn_kernarg_segment_ptr();
  asm volatile(
"s_load_dwordx8 s[36:43], %0, 0x0\n"
"s_load_dwordx4 s[44:47], %0, 0x20\n"
"s_load_dwordx2 s[48:49], %0, 0x30\n"
"s_mov_b32 s50, %1\n"
"s_mov_b32 s51, %2\n"
"s_mov_b32 s52, 0\n"
"L_setup:\n"
"v_mbcnt_lo_u32_b32 v255, -1, 0\n"
"v_mbcnt_hi_u32_b32 v255, -1, v255\n"
"s_lshr_b32 s88, s50, 3\n"
"s_lshl3_add_u32 s80, s52, s51\n"
"s_cmp_eq_u32 s80, 7\n"
"s_cbranch_scc0 L_nopf\n"
"s_getpc_b64 s[80:81]\n"
"L_pc:\n"
"v_lshl_add_u32 v1, s88, 4, v255\n"
"v_lshlrev_b32 v1, 6, v1\n"
"v_min_u32 v1, L_exit-L_pc, v1\n"
"s_mov_b64 exec, 0xffff\n"
"global_load_dword v247, v1, s[80:81]\n"
"s_mov_b64 exec, -1\n"
"L_nopf:\n"
"s_and_b32 s53, s51, 3\n"
"s_and_b32 s80, s50, 1\n"
"s_bfe_u32 s81, s50, 0x20001\n"
"s_lshl_b32 s80, s80, 2\n"
"s_lshl_b32 s81, s81, 3\n"
"s_and_b32 s82, s88, 3\n"
"s_lshr_b32 s83, s88, 2\n"
"s_add_u32 s84, s80, s82\n"
"s_add_u32 s85, s81, s83\n"
"s_lshl1_add_u32 s82, s82, s83\n"
"s_and_b32 s97, s82, 6\n"
"s_and_b32 s82, s88, 7\n"
"s_add_u32 s86, s81, s82\n"
"s_lshr_b32 s82, s88, 3\n"
"s_add_u32 s87, s80, s82\n"
"s_lshl_b32 s95, s53, 12\n"
"s_mov_b32 s93, 0x40000\n"
"v_lshlrev_b32 v254, 4, v255\n"
"s_mov_b32 s74, 15073280\n"
"s_mov_b32 s75, 0x20000\n"
"s_cmp_ge_u32 s51, 4\n"
"s_cbranch_scc1 L_compute\n"
"s_setprio 2\n"
"v_lshrrev_b32 v240, 3, v255\n"
"v_and_b32 v240, 2, v240\n"
"v_and_b32 v241, 3, v255\n"
"v_xor_b32 v240, v240, v241\n"
"v_lshlrev_b32 v240, 4, v240\n"
"v_lshrrev_b32 v241, 5, v255\n"
"v_lshl_or_b32 v240, v241, 6, v240\n"
"v_xor_b32 v241, 48, v240\n"
"v_bfe_u32 v242, v255, 2, 3\n"
"v_lshl_add_u32 v2, v242, 13, v240\n"
"v_lshl_add_u32 v4, v242, 13, v241\n"
"v_add_u32 v3, 128, v2\n"
"v_add_u32 v2, 1024, v2\n"
"v_add_u32 v5, 63616, v4\n"
"v_add_u32 v4, 64512, v4\n"
"v_lshl_add_u32 v0, v242, 10, v240\n"
"v_lshl_add_u32 v1, v242, 10, v241\n"
"v_add_u32 v1, 7168, v1\n"
"s_mul_i32 s89, s53, 49152\n"
"s_lshl_b32 s90, s53, 17\n"
"s_mul_i32 s91, s53, 6144\n"
"s_mov_b32 s58, 196608\n"
"s_mov_b32 s59, 0x20000\n"
"s_lshl_b32 s82, s85, 8\n"
"s_sub_u32 s81, 4195328, s82\n"
"s_mov_b32 s62, s81\n"
"s_mov_b32 s63, 0x20000\n"
"s_sub_u32 s82, s82, 1024\n"
"s_ashr_i32 s83, s82, 31\n"
"s_mul_i32 s96, s84, 196608\n"
"s_lshl_b32 s80, s97, 19\n"
"s_add_u32 s80, s80, s90\n"
"s_add_u32 m0, s95, 49152\n"
"s_waitcnt lgkmcnt(0)\n"
"s_add_u32 s60, s42, s82\n"
"s_addc_u32 s61, s43, s83\n"
"buffer_load_dwordx4 v2, s[60:63], s80 offen lds\n"
"buffer_load_dwordx4 v3, s[60:63], s80 offen offset:1024 lds\n"
"buffer_load_dwordx4 v4, s[60:63], s80 offen offset:2048 lds\n"
"buffer_load_dwordx4 v5, s[60:63], s80 offen offset:3072 lds\n"
"s_add_u32 s56, s36, s96\n"
"s_addc_u32 s57, s37, 0\n"
"s_lshl_b32 s81, s97, 7\n"
"s_add_u32 s81, s81, s89\n"
"s_add_u32 m0, s91, 65536\n"
"s_add_u32 s82, s81, 14336\n"
"buffer_load_dwordx4 v0, s[56:59], s81 offen lds\n"
"buffer_load_dwordx4 v1, s[56:59], s81 offen offset:1024 lds\n"
"buffer_load_dwordx4 v0, s[56:59], s82 offen offset:2048 lds\n"
"buffer_load_dwordx4 v1, s[56:59], s82 offen offset:3072 lds\n"
"s_add_u32 m0, s91, 69632\n"
"s_add_u32 s82, s81, 32768\n"
"buffer_load_dwordx4 v0, s[56:59], s82 offen lds\n"
"buffer_load_dwordx4 v1, s[56:59], s82 offen offset:1024 lds\n"
"s_add_u32 s97, s97, 1\n"
"s_and_b32 s97, s97, 7\n"
"s_lshl_b32 s80, s97, 19\n"
"s_add_u32 s80, s80, s90\n"
"s_add_u32 m0, s95, 8192\n"
"s_nop 0\n"
"buffer_load_dwordx4 v2, s[60:63], s80 offen lds\n"
"buffer_load_dwordx4 v3, s[60:63], s80 offen offset:1024 lds\n"
"buffer_load_dwordx4 v4, s[60:63], s80 offen offset:2048 lds\n"
"buffer_load_dwordx4 v5, s[60:63], s80 offen offset:3072 lds\n"
"v_lshrrev_b32 v243, 4, v255\n"
"v_lshlrev_b32 v11, 9, v243\n"
"v_and_b32 v244, 15, v255\n"
"v_lshl_add_u32 v10, v244, 2, v11\n"
"v_lshl_add_u32 v10, s53, 6, v10\n"
"v_mov_b32 v12, 9\n"
"ds_write_b32 v10, v12 offset:256\n"
"v_add_u32 v14, s95, v254\n"
"v_add_u32 v15, 32768, v14\n"
"v_lshl_add_u32 v6, v242, 8, v240\n"
"v_lshl_add_u32 v8, v242, 8, v241\n"
"v_add_u32 v7, 128, v6\n"
"v_add_u32 v6, 1024, v6\n"
"v_add_u32 v9, 128, v8\n"
"v_add_u32 v8, 1024, v8\n"
"s_waitcnt lgkmcnt(0)\n"
"s_load_dwordx2 s[54:55], s[48:49], 0x0\n"
"s_mov_b64 s[72:73], s[48:49]\n"
"s_mov_b64 s[68:69], s[44:45]\n"
"s_mov_b32 s70, 0x400000\n"
"s_mov_b32 s71, 0x20000\n"
"s_lshl_b32 s80, s84, 5\n"
"s_add_u32 s80, s80, s85\n"
"s_lshl_b32 s80, s80, 18\n"
"s_sub_u32 s80, s80, 1024\n"
"s_ashr_i32 s81, s80, 31\n"
"s_add_u32 s64, s38, s80\n"
"s_addc_u32 s65, s39, s81\n"
"s_add_u32 s40, s40, s80\n"
"s_addc_u32 s41, s41, s81\n"
"s_mov_b32 s66, 263168\n"
"s_mov_b32 s67, 0x20000\n"
"s_cmp_ge_u32 s53, 2\n"
"s_cselect_b32 s96, -4, 1\n"
"s_add_u32 s96, s96, s53\n"
"s_lshl_b32 s96, s96, 12\n"
"s_waitcnt vmcnt(4)\n"
"s_barrier\n"
"s_lshl_b32 s81, s97, 7\n"
"s_add_u32 s81, s81, s89\n"
"s_add_u32 m0, s91, 24576\n"
"s_add_u32 s82, s81, 14336\n"
"buffer_load_dwordx4 v0, s[56:59], s81 offen lds\n"
"buffer_load_dwordx4 v1, s[56:59], s81 offen offset:1024 lds\n"
"buffer_load_dwordx4 v0, s[56:59], s82 offen offset:2048 lds\n"
"buffer_load_dwordx4 v1, s[56:59], s82 offen offset:3072 lds\n"
"s_add_u32 m0, s91, 28672\n"
"s_add_u32 s82, s81, 32768\n"
"buffer_load_dwordx4 v0, s[56:59], s82 offen lds\n"
"buffer_load_dwordx4 v1, s[56:59], s82 offen offset:1024 lds\n"
"s_add_u32 s97, s97, 1\n"
"s_and_b32 s97, s97, 7\n"
"s_lshl_b32 s80, s97, 19\n"
"s_add_u32 s80, s80, s90\n"
"s_add_u32 m0, s95, 90112\n"
"s_nop 0\n"
"buffer_load_dwordx4 v2, s[60:63], s80 offen lds\n"
"buffer_load_dwordx4 v3, s[60:63], s80 offen offset:1024 lds\n"
"buffer_load_dwordx4 v4, s[60:63], s80 offen offset:2048 lds\n"
"buffer_load_dwordx4 v5, s[60:63], s80 offen offset:3072 lds\n"
"s_lshl_b32 s81, s97, 7\n"
"s_add_u32 s81, s81, s89\n"
"s_add_u32 m0, s91, 106496\n"
"s_add_u32 s82, s81, 14336\n"
"buffer_load_dwordx4 v0, s[56:59], s81 offen lds\n"
"buffer_load_dwordx4 v1, s[56:59], s81 offen offset:1024 lds\n"
"buffer_load_dwordx4 v0, s[56:59], s82 offen offset:2048 lds\n"
"buffer_load_dwordx4 v1, s[56:59], s82 offen offset:3072 lds\n"
"s_add_u32 m0, s91, 110592\n"
"s_add_u32 s82, s81, 32768\n"
"buffer_load_dwordx4 v0, s[56:59], s82 offen lds\n"
"buffer_load_dwordx4 v1, s[56:59], s82 offen offset:1024 lds\n"
"s_add_u32 s97, s97, 1\n"
"s_and_b32 s97, s97, 7\n"
"s_cmp_eq_u32 s53, 0\n"
"s_cbranch_scc0 L_cpl\n"
"v_lshrrev_b32 v20, 7, v254\n"
"v_and_b32 v21, 0x70, v254\n"
"v_lshl_or_b32 v20, v20, 10, v21\n"
"s_lshl_b32 s80, s50, 13\n"
"buffer_load_dwordx4 v[16:19], v20, s[68:71], s80 offen offset:384\n"
"L_cpl:\n"
"s_waitcnt lgkmcnt(0)\n"
"s_mul_i32 s80, s54, 0x4c957f2d\n"
"s_mul_hi_u32 s81, s54, 0x4c957f2d\n"
"s_mul_i32 s82, s54, 0x5851f42d\n"
"s_mul_i32 s83, s55, 0x4c957f2d\n"
"s_add_u32 s81, s81, s82\n"
"s_add_u32 s81, s81, s83\n"
"s_add_u32 s54, s80, 0xf767814f\n"
"s_addc_u32 s55, s81, 0x14057b7e\n"
"v_mov_b32 v250, s54\n"
"v_mov_b32 v251, s55\n"
"s_waitcnt vmcnt(10)\n"
"s_barrier\n"
"s_cmp_eq_u32 s53, 2\n"
"s_cselect_b32 s80, 0, s96\n"
"s_min_u32 s80, s80, 0x100000\n"
"s_add_u32 m0, s95, 147456\n"
"s_nop 0\n"
"buffer_load_dwordx4 v6, s[64:67], s80 offen nt lds\n"
"buffer_load_dwordx4 v7, s[64:67], s80 offen offset:1024 nt lds\n"
"buffer_load_dwordx4 v8, s[64:67], s80 offen offset:2048 nt lds\n"
"buffer_load_dwordx4 v9, s[64:67], s80 offen offset:3072 nt lds\n"
"s_cmp_ge_u32 s53, 2\n"
"s_cselect_b64 s[64:65], s[40:41], s[64:65]\n"
"s_add_u32 s96, s96, 8192\n"
"s_lshl_b32 s80, s97, 19\n"
"s_add_u32 s80, s80, s90\n"
"s_add_u32 m0, s95, 49152\n"
"s_nop 0\n"
"buffer_load_dwordx4 v2, s[60:63], s80 offen lds\n"
"buffer_load_dwordx4 v3, s[60:63], s80 offen offset:1024 lds\n"
"buffer_load_dwordx4 v4, s[60:63], s80 offen offset:2048 lds\n"
"buffer_load_dwordx4 v5, s[60:63], s80 offen offset:3072 lds\n"
"s_lshl_b32 s81, s97, 7\n"
"s_add_u32 s81, s81, s89\n"
"s_add_u32 m0, s91, 65536\n"
"s_add_u32 s82, s81, 14336\n"
"buffer_load_dwordx4 v0, s[56:59], s81 offen lds\n"
"buffer_load_dwordx4 v1, s[56:59], s81 offen offset:1024 lds\n"
"buffer_load_dwordx4 v0, s[56:59], s82 offen offset:2048 lds\n"
"buffer_load_dwordx4 v1, s[56:59], s82 offen offset:3072 lds\n"
"s_add_u32 m0, s91, 69632\n"
"s_add_u32 s82, s81, 32768\n"
"buffer_load_dwordx4 v0, s[56:59], s82 offen lds\n"
"buffer_load_dwordx4 v1, s[56:59], s82 offen offset:1024 lds\n"
"s_add_u32 s97, s97, 1\n"
"s_and_b32 s97, s97, 7\n"
"s_waitcnt vmcnt(14)\n"
"s_barrier\n"
"s_lshl_b32 s80, s97, 19\n"
"s_add_u32 s80, s80, s90\n"
"s_add_u32 m0, s95, 8192\n"
"s_nop 0\n"
"buffer_load_dwordx4 v2, s[60:63], s80 offen lds\n"
"buffer_load_dwordx4 v3, s[60:63], s80 offen offset:1024 lds\n"
"buffer_load_dwordx4 v4, s[60:63], s80 offen offset:2048 lds\n"
"buffer_load_dwordx4 v5, s[60:63], s80 offen offset:3072 lds\n"
"s_lshl_b32 s81, s97, 7\n"
"s_add_u32 s81, s81, s89\n"
"s_add_u32 m0, s91, 24576\n"
"s_add_u32 s82, s81, 14336\n"
"buffer_load_dwordx4 v0, s[56:59], s81 offen lds\n"
"buffer_load_dwordx4 v1, s[56:59], s81 offen offset:1024 lds\n"
"buffer_load_dwordx4 v0, s[56:59], s82 offen offset:2048 lds\n"
"buffer_load_dwordx4 v1, s[56:59], s82 offen offset:3072 lds\n"
"s_add_u32 m0, s91, 28672\n"
"s_add_u32 s82, s81, 32768\n"
"buffer_load_dwordx4 v0, s[56:59], s82 offen lds\n"
"buffer_load_dwordx4 v1, s[56:59], s82 offen offset:1024 lds\n"
"s_add_u32 s97, s97, 1\n"
"s_and_b32 s97, s97, 7\n"
"s_waitcnt vmcnt(10)\n"
"s_barrier\n"
"s_add_u32 m0, s95, 131072\n"
"s_nop 0\n"
"buffer_load_dwordx4 v6, s[64:67], s96 offen nt lds\n"
"buffer_load_dwordx4 v7, s[64:67], s96 offen offset:1024 nt lds\n"
"buffer_load_dwordx4 v8, s[64:67], s96 offen offset:2048 nt lds\n"
"buffer_load_dwordx4 v9, s[64:67], s96 offen offset:3072 nt lds\n"
"s_add_u32 s96, s96, 8192\n"
"s_lshl_b32 s80, s97, 19\n"
"s_add_u32 s80, s80, s90\n"
"s_add_u32 m0, s95, 90112\n"
"s_nop 0\n"
"buffer_load_dwordx4 v2, s[60:63], s80 offen lds\n"
"buffer_load_dwordx4 v3, s[60:63], s80 offen offset:1024 lds\n"
"buffer_load_dwordx4 v4, s[60:63], s80 offen offset:2048 lds\n"
"buffer_load_dwordx4 v5, s[60:63], s80 offen offset:3072 lds\n"
"s_lshl_b32 s81, s97, 7\n"
"s_add_u32 s81, s81, s89\n"
"s_add_u32 m0, s91, 106496\n"
"s_add_u32 s82, s81, 14336\n"
"buffer_load_dwordx4 v0, s[56:59], s81 offen lds\n"
"buffer_load_dwordx4 v1, s[56:59], s81 offen offset:1024 lds\n"
"buffer_load_dwordx4 v0, s[56:59], s82 offen offset:2048 lds\n"
"buffer_load_dwordx4 v1, s[56:59], s82 offen offset:3072 lds\n"
"s_add_u32 m0, s91, 110592\n"
"s_add_u32 s82, s81, 32768\n"
"buffer_load_dwordx4 v0, s[56:59], s82 offen lds\n"
"buffer_load_dwordx4 v1, s[56:59], s82 offen offset:1024 lds\n"
"s_add_u32 s97, s97, 1\n"
"s_and_b32 s97, s97, 7\n"
"s_cmp_eq_u32 s53, 0\n"
"s_cbranch_scc0 L_cps\n"
"s_lshl_b32 s80, s50, 13\n"
"s_add_u32 s80, s80, 12976128\n"
"buffer_store_dwordx4 v[16:19], v20, s[72:75], s80 offen sc1\n"
"L_cps:\n"
"s_waitcnt vmcnt(14)\n"
"s_barrier\n"
"s_lshl_b32 s80, s97, 19\n"
"s_add_u32 s80, s80, s90\n"
"s_add_u32 m0, s95, 49152\n"
"s_nop 0\n"
"buffer_load_dwordx4 v2, s[60:63], s80 offen lds\n"
"buffer_load_dwordx4 v3, s[60:63], s80 offen offset:1024 lds\n"
"buffer_load_dwordx4 v4, s[60:63], s80 offen offset:2048 lds\n"
"buffer_load_dwordx4 v5, s[60:63], s80 offen offset:3072 lds\n"
"s_lshl_b32 s81, s97, 7\n"
"s_add_u32 s81, s81, s89\n"
"s_add_u32 m0, s91, 65536\n"
"s_add_u32 s82, s81, 14336\n"
"buffer_load_dwordx4 v0, s[56:59], s81 offen lds\n"
"buffer_load_dwordx4 v1, s[56:59], s81 offen offset:1024 lds\n"
"buffer_load_dwordx4 v0, s[56:59], s82 offen offset:2048 lds\n"
"buffer_load_dwordx4 v1, s[56:59], s82 offen offset:3072 lds\n"
"s_add_u32 m0, s91, 69632\n"
"s_add_u32 s82, s81, 32768\n"
"buffer_load_dwordx4 v0, s[56:59], s82 offen lds\n"
"buffer_load_dwordx4 v1, s[56:59], s82 offen offset:1024 lds\n"
"s_add_u32 s97, s97, 1\n"
"s_and_b32 s97, s97, 7\n"
"s_waitcnt vmcnt(10)\n"
"s_barrier\n"
"s_lshl_b32 s80, s97, 19\n"
"s_add_u32 s80, s80, s90\n"
"s_add_u32 m0, s95, 8192\n"
"s_nop 0\n"
"buffer_load_dwordx4 v2, s[60:63], s80 offen lds\n"
"buffer_load_dwordx4 v3, s[60:63], s80 offen offset:1024 lds\n"
"buffer_load_dwordx4 v4, s[60:63], s80 offen offset:2048 lds\n"
"buffer_load_dwordx4 v5, s[60:63], s80 offen offset:3072 lds\n"
"s_lshl_b32 s81, s97, 7\n"
"s_add_u32 s81, s81, s89\n"
"s_add_u32 m0, s91, 24576\n"
"s_add_u32 s82, s81, 14336\n"
"buffer_load_dwordx4 v0, s[56:59], s81 offen lds\n"
"buffer_load_dwordx4 v1, s[56:59], s81 offen offset:1024 lds\n"
"buffer_load_dwordx4 v0, s[56:59], s82 offen offset:2048 lds\n"
"buffer_load_dwordx4 v1, s[56:59], s82 offen offset:3072 lds\n"
"s_add_u32 m0, s91, 28672\n"
"s_add_u32 s82, s81, 32768\n"
"buffer_load_dwordx4 v0, s[56:59], s82 offen lds\n"
"buffer_load_dwordx4 v1, s[56:59], s82 offen offset:1024 lds\n"
"s_add_u32 s97, s97, 1\n"
"s_and_b32 s97, s97, 7\n"
"s_lshl_b32 s80, s87, 7\n"
"s_mul_i32 s81, s86, 49152\n"
"s_add_u32 s80, s80, s81\n"
"s_add_u32 s76, s46, s80\n"
"s_addc_u32 s77, s47, 0\n"
"s_sub_u32 s81, 1572864, s80\n"
"s_mov_b32 s78, s81\n"
"s_mov_b32 s79, 0x20000\n"
"s_bfe_u32 s80, s88, 0x10002\n"
"s_lshl1_add_u32 s80, s80, s51\n"
"s_and_b32 s80, s80, 3\n"
"s_and_b32 s81, s88, 3\n"
"s_lshl2_add_u32 s80, s81, s80\n"
"s_xor_b32 s82, s80, 0\n"
"s_lshl_b32 s83, s82, 10\n"
"s_add_u32 s83, s83, s87\n"
"s_lshl_b32 s56, s83, 7\n"
"s_lshl_b32 s83, s86, 5\n"
"s_add_u32 s83, s83, s82\n"
"s_lshl_b32 s81, s83, 7\n"
"s_add_u32 s81, s81, 12845056\n"
"v_mov_b32 v253, s81\n"
"s_mul_i32 s83, s83, 12288\n"
"s_add_u32 s60, s83, 262144\n"
"s_xor_b32 s82, s80, 4\n"
"s_lshl_b32 s83, s82, 10\n"
"s_add_u32 s83, s83, s87\n"
"s_lshl_b32 s57, s83, 7\n"
"s_lshl_b32 s83, s86, 5\n"
"s_add_u32 s83, s83, s82\n"
"s_lshl_b32 s81, s83, 7\n"
"s_add_u32 s81, s81, 12845056\n"
"v_writelane_b32 v253, s81, 1\n"
"s_mul_i32 s83, s83, 12288\n"
"s_add_u32 s61, s83, 262144\n"
"s_xor_b32 s82, s80, 8\n"
"s_lshl_b32 s83, s82, 10\n"
"s_add_u32 s83, s83, s87\n"
"s_lshl_b32 s58, s83, 7\n"
"s_lshl_b32 s83, s86, 5\n"
"s_add_u32 s83, s83, s82\n"
"s_lshl_b32 s81, s83, 7\n"
"s_add_u32 s81, s81, 12845056\n"
"v_writelane_b32 v253, s81, 2\n"
"s_mul_i32 s83, s83, 12288\n"
"s_add_u32 s62, s83, 262144\n"
"s_xor_b32 s82, s80, 12\n"
"s_lshl_b32 s83, s82, 10\n"
"s_add_u32 s83, s83, s87\n"
"s_lshl_b32 s59, s83, 7\n"
"s_lshl_b32 s83, s86, 5\n"
"s_add_u32 s83, s83, s82\n"
"s_lshl_b32 s81, s83, 7\n"
"s_add_u32 s81, s81, 12845056\n"
"v_writelane_b32 v253, s81, 3\n"
"s_mul_i32 s83, s83, 12288\n"
"s_add_u32 s63, s83, 262144\n"
"v_lshrrev_b32 v243, 4, v255\n"
"v_lshlrev_b32 v244, 1, v243\n"
"v_mov_b32 v245, 0x78\n"
"v_lshrrev_b32 v244, v244, v245\n"
"v_bfe_u32 v245, v255, 1, 1\n"
"v_xor_b32 v244, v244, v245\n"
"v_and_b32 v244, 3, v244\n"
"v_lshlrev_b32 v244, 4, v244\n"
"v_and_b32 v245, 1, v255\n"
"v_lshl_or_b32 v244, v245, 3, v244\n"
"v_bfe_u32 v245, v255, 2, 3\n"
"v_lshl_or_b32 v244, v245, 6, v244\n"
"v_lshrrev_b32 v245, 5, v255\n"
"v_lshl_or_b32 v255, v245, 10, v244\n"
"s_add_u32 s80, s95, 16384\n"
"v_add_u32 v255, s80, v255\n"
"s_mov_b64 s[36:37], s[68:69]\n"
"s_mov_b64 s[38:39], s[70:71]\n"
"v_mov_b32 v240, v255\n"
"v_xor_b32 v241, 32, v240\n"
"s_add_u32 s81, s56, 14336\n"
"buffer_load_dwordx4 v[48:51], v0, s[36:39], s56 offen sc1\n"
"buffer_load_dwordx4 v[52:55], v1, s[36:39], s56 offen offset:1024 sc1\n"
"buffer_load_dwordx4 v[56:59], v0, s[36:39], s81 offen offset:2048 sc1\n"
"buffer_load_dwordx4 v[60:63], v1, s[36:39], s81 offen offset:3072 sc1\n"
"s_waitcnt vmcnt(14)\n"
"s_barrier\n"
"s_add_u32 m0, s95, 114688\n"
"s_nop 0\n"
"buffer_load_dwordx4 v6, s[64:67], s96 offen nt lds\n"
"buffer_load_dwordx4 v7, s[64:67], s96 offen offset:1024 nt lds\n"
"buffer_load_dwordx4 v8, s[64:67], s96 offen offset:2048 nt lds\n"
"buffer_load_dwordx4 v9, s[64:67], s96 offen offset:3072 nt lds\n"
"s_add_u32 s96, s96, 8192\n"
"s_waitcnt vmcnt(8)\n"
"s_barrier\n"
"s_memrealtime s[82:83]\n"
"s_waitcnt lgkmcnt(0)\n"
"s_mul_i32 s94, s82, -4\n"
"s_sub_u32 s94, s94, 244\n"
"s_mov_b32 s92, s94\n"
"s_add_u32 m0, s95, 98304\n"
"s_nop 0\n"
"buffer_load_dwordx4 v6, s[64:67], s96 offen nt lds\n"
"buffer_load_dwordx4 v7, s[64:67], s96 offen offset:1024 nt lds\n"
"buffer_load_dwordx4 v8, s[64:67], s96 offen offset:2048 nt lds\n"
"buffer_load_dwordx4 v9, s[64:67], s96 offen offset:3072 nt lds\n"
"s_add_u32 s96, s96, 8192\n"
"s_lshl2_add_u32 s80, s50, s53\n"
"s_lshl3_add_u32 s80, s80, 4096\n"
"s_mov_b64 exec, 1\n"
"buffer_store_dwordx2 v[250:251], off, s[72:75], s80 sc1\n"
"s_mov_b64 exec, -1\n"
"s_call_b64 s[98:99], L_hpace\n"
"s_add_u32 m0, s95, 81920\n"
"s_nop 0\n"
"buffer_load_dwordx4 v6, s[64:67], s96 offen nt lds\n"
"buffer_load_dwordx4 v7, s[64:67], s96 offen offset:1024 nt lds\n"
"buffer_load_dwordx4 v8, s[64:67], s96 offen offset:2048 nt lds\n"
"buffer_load_dwordx4 v9, s[64:67], s96 offen offset:3072 nt lds\n"
"s_add_u32 s96, s96, 8192\n"
"s_sub_u32 s94, s94, 5\n"
"s_sub_u32 s92, s92, 5\n"
"s_waitcnt vmcnt(9)\n"
"v_max_u32 v12, 10, v12\n"
"ds_write_b32 v10, v12 offset:256\n"
"s_waitcnt lgkmcnt(0)\n"
"s_call_b64 s[98:99], L_hpace\n"
"s_add_u32 m0, s95, 65536\n"
"s_nop 0\n"
"buffer_load_dwordx4 v6, s[64:67], s96 offen nt lds\n"
"buffer_load_dwordx4 v7, s[64:67], s96 offen offset:1024 nt lds\n"
"buffer_load_dwordx4 v8, s[64:67], s96 offen offset:2048 nt lds\n"
"buffer_load_dwordx4 v9, s[64:67], s96 offen offset:3072 nt lds\n"
"s_add_u32 s96, s96, 8192\n"
"s_sub_u32 s94, s94, 3\n"
"s_sub_u32 s92, s92, 3\n"
"s_waitcnt vmcnt(9)\n"
"v_max_u32 v12, 11, v12\n"
"ds_write_b32 v10, v12 offset:256\n"
"s_add_u32 s80, s59, 65536\n"
"buffer_load_dwordx4 v[224:227], v0, s[36:39], s80 offen sc1\n"
"buffer_load_dwordx4 v[228:231], v1, s[36:39], s80 offen offset:1024 sc1\n"
"s_sub_u32 s94, s94, 59\n"
"s_sub_u32 s92, s92, 59\n"
"s_waitcnt lgkmcnt(0)\n"
"s_call_b64 s[98:99], L_hpace\n"
"s_add_u32 m0, s95, 49152\n"
"s_nop 0\n"
"buffer_load_dwordx4 v6, s[64:67], s96 offen nt lds\n"
"buffer_load_dwordx4 v7, s[64:67], s96 offen offset:1024 nt lds\n"
"buffer_load_dwordx4 v8, s[64:67], s96 offen offset:2048 nt lds\n"
"buffer_load_dwordx4 v9, s[64:67], s96 offen offset:3072 nt lds\n"
"s_add_u32 s96, s96, 8192\n"
"s_cmp_eq_u32 s87, 3\n"
"s_cbranch_scc0 L_cpa25\n"
"s_lshr_b32 s80, s56, 17\n"
"s_lshl_b32 s80, s80, 9\n"
"v_and_b32 v20, 0x300, v254\n"
"v_lshlrev_b32 v20, 3, v20\n"
"v_xor_b32 v20, s80, v20\n"
"v_and_b32 v21, 0xf0, v254\n"
"v_lshl_or_b32 v20, v21, 1, v20\n"
"s_movk_i32 s80, 4096\n"
"buffer_load_dwordx2 v[22:23], v20, s[72:75], s80 offen sc1\n"
"L_cpa25:\n"
"ds_write_b32 v10, v250 offset:2048\n"
"ds_write_b32 v10, v251 offset:2304\n"
"s_waitcnt vmcnt(10)\n"
"v_max_u32 v12, 12, v12\n"
"ds_write_b32 v10, v12 offset:256\n"
"s_add_u32 s81, s59, 79872\n"
"buffer_load_dwordx4 v[232:235], v0, s[36:39], s81 offen offset:2048 sc1\n"
"buffer_load_dwordx4 v[236:239], v1, s[36:39], s81 offen offset:3072 sc1\n"
"s_sub_u32 s94, s94, 59\n"
"s_sub_u32 s92, s92, 59\n"
"ds_read_b32 v13, v11\n"
"s_waitcnt lgkmcnt(0)\n"
"v_cmp_gt_u32 vcc, 8, v13\n"
"s_cbranch_vccz L_fr0\n"
"s_movk_i32 s80, 8\n"
"s_call_b64 s[98:99], L_spinFR\n"
"L_fr0:\n"
"s_call_b64 s[98:99], L_hpace\n"
"s_add_u32 m0, s95, 32768\n"
"s_nop 0\n"
"buffer_load_dwordx4 v6, s[64:67], s96 offen nt lds\n"
"buffer_load_dwordx4 v7, s[64:67], s96 offen offset:1024 nt lds\n"
"buffer_load_dwordx4 v8, s[64:67], s96 offen offset:2048 nt lds\n"
"buffer_load_dwordx4 v9, s[64:67], s96 offen offset:3072 nt lds\n"
"s_add_u32 s96, s96, 8192\n"
"s_sub_u32 s94, s94, -1\n"
"s_sub_u32 s92, s92, -1\n"
"ds_write_b128 v14, v[48:51] offset:16384\n"
"ds_write_b128 v14, v[52:55] offset:17408\n"
"ds_write_b128 v14, v[56:59] offset:18432\n"
"ds_write_b128 v14, v[60:63] offset:19456\n"
"ds_read_b64_tr_b16 v[48:49], v240\n"
"ds_read_b64_tr_b16 v[50:51], v240 offset:2048\n"
"ds_read_b64_tr_b16 v[52:53], v241\n"
"ds_read_b64_tr_b16 v[54:55], v241 offset:2048\n"
"ds_read_b64_tr_b16 v[56:57], v240 offset:512\n"
"ds_read_b64_tr_b16 v[58:59], v240 offset:2560\n"
"ds_read_b64_tr_b16 v[60:61], v241 offset:512\n"
"ds_read_b64_tr_b16 v[62:63], v241 offset:2560\n"
"s_waitcnt vmcnt(12)\n"
"v_max_u32 v12, 13, v12\n"
"ds_write_b32 v10, v12 offset:256\n"
"s_waitcnt lgkmcnt(0)\n"
"s_add_u32 s80, s56, 32768\n"
"s_add_u32 m0, s95, 16384\n"
"s_nop 0\n"
"buffer_load_dwordx4 v0, s[36:39], s80 offen sc1 lds\n"
"buffer_load_dwordx4 v1, s[36:39], s80 offen offset:1024 sc1 lds\n"
"s_sub_u32 s94, s94, 59\n"
"s_sub_u32 s92, s92, 59\n"
"ds_read_b32 v13, v11\n"
"s_waitcnt lgkmcnt(0)\n"
"v_cmp_gt_u32 vcc, 9, v13\n"
"s_cbranch_vccz L_fr1\n"
"s_movk_i32 s80, 9\n"
"s_call_b64 s[98:99], L_spinFR\n"
"L_fr1:\n"
"s_call_b64 s[98:99], L_hpace\n"
"s_add_u32 m0, s95, 147456\n"
"s_nop 0\n"
"buffer_load_dwordx4 v6, s[64:67], s96 offen nt lds\n"
"buffer_load_dwordx4 v7, s[64:67], s96 offen offset:1024 nt lds\n"
"buffer_load_dwordx4 v8, s[64:67], s96 offen offset:2048 nt lds\n"
"buffer_load_dwordx4 v9, s[64:67], s96 offen offset:3072 nt lds\n"
"s_add_u32 s96, s96, 8192\n"
"s_sub_u32 s94, s94, -3\n"
"s_sub_u32 s92, s92, -3\n"
"s_waitcnt vmcnt(12)\n"
"v_max_u32 v12, 14, v12\n"
"ds_write_b32 v10, v12 offset:256\n"
"s_waitcnt lgkmcnt(0)\n"
"s_add_u32 s81, s56, 47104\n"
"s_add_u32 m0, s95, 16384\n"
"s_nop 0\n"
"buffer_load_dwordx4 v0, s[36:39], s81 offen offset:2048 sc1 lds\n"
"buffer_load_dwordx4 v1, s[36:39], s81 offen offset:3072 sc1 lds\n"
"s_sub_u32 s94, s94, 59\n"
"s_sub_u32 s92, s92, 59\n"
"ds_read_b32 v13, v11\n"
"s_waitcnt lgkmcnt(0)\n"
"v_cmp_gt_u32 vcc, 10, v13\n"
"s_cbranch_vccz L_fr2\n"
"s_movk_i32 s80, 10\n"
"s_call_b64 s[98:99], L_spinFR\n"
"L_fr2:\n"
"s_call_b64 s[98:99], L_hpace\n"
"s_add_u32 m0, s95, 131072\n"
"s_nop 0\n"
"buffer_load_dwordx4 v6, s[64:67], s96 offen nt lds\n"
"buffer_load_dwordx4 v7, s[64:67], s96 offen offset:1024 nt lds\n"
"buffer_load_dwordx4 v8, s[64:67], s96 offen offset:2048 nt lds\n"
"buffer_load_dwordx4 v9, s[64:67], s96 offen offset:3072 nt lds\n"
"s_add_u32 s96, s96, 8192\n"
"s_sub_u32 s94, s94, -4\n"
"s_sub_u32 s92, s92, -4\n"
"s_waitcnt vmcnt(12)\n"
"v_max_u32 v12, 15, v12\n"
"ds_write_b32 v10, v12 offset:256\n"
"s_waitcnt vmcnt(10)\n"
"ds_read_b64_tr_b16 v[64:65], v240\n"
"ds_read_b64_tr_b16 v[68:69], v241\n"
"ds_read_b64_tr_b16 v[72:73], v240 offset:512\n"
"ds_read_b64_tr_b16 v[76:77], v241 offset:512\n"
"s_cmp_eq_u32 s87, 3\n"
"s_cbranch_scc0 L_cpu26\n"
"v_cmp_ne_u64 vcc, s[54:55], v[22:23]\n"
"s_add_u32 s80, s48, 12975744\n"
"s_addc_u32 s81, s49, 0\n"
"s_cmp_eq_u64 vcc, 0\n"
"s_cselect_b64 s[36:37], s[80:81], s[36:37]\n"
"L_cpu26:\n"
"s_waitcnt lgkmcnt(0)\n"
"s_add_u32 s80, s56, 65536\n"
"s_add_u32 m0, s95, 16384\n"
"s_nop 0\n"
"buffer_load_dwordx4 v0, s[36:39], s80 offen sc1 lds\n"
"buffer_load_dwordx4 v1, s[36:39], s80 offen offset:1024 sc1 lds\n"
"ds_read_b32 v13, v11\n"
"s_waitcnt lgkmcnt(0)\n"
"v_cmp_gt_u32 vcc, 11, v13\n"
"s_cbranch_vccz L_fr3\n"
"s_movk_i32 s80, 11\n"
"s_call_b64 s[98:99], L_spinFR\n"
"L_fr3:\n"
"s_call_b64 s[98:99], L_hpace\n"
"s_add_u32 m0, s95, 114688\n"
"s_nop 0\n"
"buffer_load_dwordx4 v6, s[64:67], s96 offen nt lds\n"
"buffer_load_dwordx4 v7, s[64:67], s96 offen offset:1024 nt lds\n"
"buffer_load_dwordx4 v8, s[64:67], s96 offen offset:2048 nt lds\n"
"buffer_load_dwordx4 v9, s[64:67], s96 offen offset:3072 nt lds\n"
"s_add_u32 s96, s96, 8192\n"
"s_sub_u32 s94, s94, -5\n"
"s_sub_u32 s92, s92, -5\n"
"s_cmp_eq_u32 s87, 3\n"
"s_cbranch_scc0 L_cpa33\n"
"s_lshr_b32 s80, s56, 17\n"
"s_lshl_b32 s80, s80, 9\n"
"v_and_b32 v20, 0x300, v254\n"
"v_lshlrev_b32 v20, 3, v20\n"
"v_xor_b32 v20, s80, v20\n"
"v_and_b32 v21, 0xf0, v254\n"
"v_lshl_or_b32 v20, v21, 1, v20\n"
"s_movk_i32 s80, 4096\n"
"buffer_load_dwordx2 v[22:23], v20, s[72:75], s80 offen sc1\n"
"L_cpa33:\n"
"s_or_b32 s80, s50, s51\n"
"s_cbranch_scc1 L_noep0\n"
"s_mov_b32 s80, 4096\n"
"s_mov_b32 s81, 8192\n"
"buffer_load_dwordx4 v[16:19], v254, s[72:75], s80 offen sc1\n"
"buffer_load_dwordx4 v[20:23], v254, s[72:75], s80 offen offset:1024 sc1\n"
"buffer_load_dwordx4 v[24:27], v254, s[72:75], s80 offen offset:2048 sc1\n"
"buffer_load_dwordx4 v[28:31], v254, s[72:75], s80 offen offset:3072 sc1\n"
"buffer_load_dwordx4 v[32:35], v254, s[72:75], s81 offen sc1\n"
"buffer_load_dwordx4 v[36:39], v254, s[72:75], s81 offen offset:1024 sc1\n"
"buffer_load_dwordx4 v[40:43], v254, s[72:75], s81 offen offset:2048 sc1\n"
"buffer_load_dwordx4 v[44:47], v254, s[72:75], s81 offen offset:3072 sc1\n"
"L_noep0:\n"
"s_waitcnt vmcnt(12)\n"
"v_max_u32 v12, 16, v12\n"
"ds_write_b32 v10, v12 offset:256\n"
"s_waitcnt vmcnt(10)\n"
"ds_read_b64_tr_b16 v[66:67], v240 offset:2048\n"
"ds_read_b64_tr_b16 v[70:71], v241 offset:2048\n"
"ds_read_b64_tr_b16 v[74:75], v240 offset:2560\n"
"ds_read_b64_tr_b16 v[78:79], v241 offset:2560\n"
"s_waitcnt lgkmcnt(0)\n"
"s_add_u32 s81, s56, 79872\n"
"s_add_u32 m0, s95, 16384\n"
"s_nop 0\n"
"buffer_load_dwordx4 v0, s[36:39], s81 offen offset:2048 sc1 lds\n"
"buffer_load_dwordx4 v1, s[36:39], s81 offen offset:3072 sc1 lds\n"
"ds_read_b32 v13, v11\n"
"s_waitcnt lgkmcnt(0)\n"
"v_cmp_gt_u32 vcc, 12, v13\n"
"s_cbranch_vccz L_fr4\n"
"s_movk_i32 s80, 12\n"
"s_call_b64 s[98:99], L_spinFR\n"
"L_fr4:\n"
"s_call_b64 s[98:99], L_hpace\n"
"s_add_u32 m0, s95, 98304\n"
"s_nop 0\n"
"buffer_load_dwordx4 v6, s[64:67], s96 offen nt lds\n"
"buffer_load_dwordx4 v7, s[64:67], s96 offen offset:1024 nt lds\n"
"buffer_load_dwordx4 v8, s[64:67], s96 offen offset:2048 nt lds\n"
"buffer_load_dwordx4 v9, s[64:67], s96 offen offset:3072 nt lds\n"
"s_add_u32 s96, s96, 8192\n"
"s_sub_u32 s94, s94, -6\n"
"s_sub_u32 s92, s92, -6\n"
"s_waitcnt vmcnt(12)\n"
"v_max_u32 v12, 17, v12\n"
"ds_write_b32 v10, v12 offset:256\n"
"s_waitcnt vmcnt(10)\n"
"ds_read_b64_tr_b16 v[80:81], v240\n"
"ds_read_b64_tr_b16 v[84:85], v241\n"
"ds_read_b64_tr_b16 v[88:89], v240 offset:512\n"
"ds_read_b64_tr_b16 v[92:93], v241 offset:512\n"
"s_waitcnt lgkmcnt(0)\n"
"s_add_u32 s80, s56, 98304\n"
"s_add_u32 m0, s95, 16384\n"
"s_nop 0\n"
"buffer_load_dwordx4 v0, s[36:39], s80 offen sc1 lds\n"
"buffer_load_dwordx4 v1, s[36:39], s80 offen offset:1024 sc1 lds\n"
"s_sub_u32 s94, s94, 59\n"
"s_sub_u32 s92, s92, 59\n"
"ds_read_b32 v13, v11\n"
"s_waitcnt lgkmcnt(0)\n"
"v_cmp_gt_u32 vcc, 13, v13\n"
"s_cbranch_vccz L_fr5\n"
"s_movk_i32 s80, 13\n"
"s_call_b64 s[98:99], L_spinFR\n"
"L_fr5:\n"
"s_call_b64 s[98:99], L_hpace\n"
"s_add_u32 m0, s95, 81920\n"
"s_nop 0\n"
"buffer_load_dwordx4 v6, s[64:67], s96 offen nt lds\n"
"buffer_load_dwordx4 v7, s[64:67], s96 offen offset:1024 nt lds\n"
"buffer_load_dwordx4 v8, s[64:67], s96 offen offset:2048 nt lds\n"
"buffer_load_dwordx4 v9, s[64:67], s96 offen offset:3072 nt lds\n"
"s_add_u32 s96, s96, 8192\n"
"s_sub_u32 s94, s94, -7\n"
"s_sub_u32 s92, s92, -7\n"
"s_waitcnt vmcnt(12)\n"
"v_max_u32 v12, 18, v12\n"
"ds_write_b32 v10, v12 offset:256\n"
"s_waitcnt vmcnt(10)\n"
"ds_read_b64_tr_b16 v[82:83], v240 offset:2048\n"
"ds_read_b64_tr_b16 v[86:87], v241 offset:2048\n"
"ds_read_b64_tr_b16 v[90:91], v240 offset:2560\n"
"ds_read_b64_tr_b16 v[94:95], v241 offset:2560\n"
"s_cmp_eq_u32 s87, 3\n"
"s_cbranch_scc0 L_cpu34\n"
"v_cmp_ne_u64 vcc, s[54:55], v[22:23]\n"
"s_add_u32 s80, s48, 12975744\n"
"s_addc_u32 s81, s49, 0\n"
"s_cmp_eq_u64 vcc, 0\n"
"s_cselect_b64 s[36:37], s[80:81], s[36:37]\n"
"L_cpu34:\n"
"s_waitcnt lgkmcnt(0)\n"
"s_add_u32 s81, s56, 112640\n"
"s_add_u32 m0, s95, 16384\n"
"s_nop 0\n"
"buffer_load_dwordx4 v0, s[36:39], s81 offen offset:2048 sc1 lds\n"
"buffer_load_dwordx4 v1, s[36:39], s81 offen offset:3072 sc1 lds\n"
"s_sub_u32 s94, s94, 59\n"
"s_sub_u32 s92, s92, 59\n"
"ds_read_b32 v13, v11\n"
"s_waitcnt lgkmcnt(0)\n"
"v_cmp_gt_u32 vcc, 14, v13\n"
"s_cbranch_vccz L_fr6\n"
"s_movk_i32 s80, 14\n"
"s_call_b64 s[98:99], L_spinFR\n"
"L_fr6:\n"
"s_call_b64 s[98:99], L_hpace\n"
"s_add_u32 m0, s95, 65536\n"
"s_nop 0\n"
"buffer_load_dwordx4 v6, s[64:67], s96 offen nt lds\n"
"buffer_load_dwordx4 v7, s[64:67], s96 offen offset:1024 nt lds\n"
"buffer_load_dwordx4 v8, s[64:67], s96 offen offset:2048 nt lds\n"
"buffer_load_dwordx4 v9, s[64:67], s96 offen offset:3072 nt lds\n"
"s_add_u32 s96, s96, 8192\n"
"s_sub_u32 s94, s94, -8\n"
"s_sub_u32 s92, s92, -8\n"
"s_waitcnt vmcnt(12)\n"
"v_max_u32 v12, 19, v12\n"
"ds_write_b32 v10, v12 offset:256\n"
"s_waitcnt vmcnt(10)\n"
"ds_read_b64_tr_b16 v[96:97], v240\n"
"ds_read_b64_tr_b16 v[100:101], v241\n"
"ds_read_b64_tr_b16 v[104:105], v240 offset:512\n"
"ds_read_b64_tr_b16 v[108:109], v241 offset:512\n"
"s_waitcnt lgkmcnt(0)\n"
"s_add_u32 m0, s95, 16384\n"
"s_nop 0\n"
"buffer_load_dwordx4 v0, s[36:39], s57 offen sc1 lds\n"
"buffer_load_dwordx4 v1, s[36:39], s57 offen offset:1024 sc1 lds\n"
"ds_read_b32 v13, v11\n"
"s_waitcnt lgkmcnt(0)\n"
"v_cmp_gt_u32 vcc, 15, v13\n"
"s_cbranch_vccz L_fr7\n"
"s_movk_i32 s80, 15\n"
"s_call_b64 s[98:99], L_spinFR\n"
"L_fr7:\n"
"s_call_b64 s[98:99], L_hpace\n"
"s_add_u32 m0, s95, 49152\n"
"s_nop 0\n"
"buffer_load_dwordx4 v6, s[64:67], s96 offen nt lds\n"
"buffer_load_dwordx4 v7, s[64:67], s96 offen offset:1024 nt lds\n"
"buffer_load_dwordx4 v8, s[64:67], s96 offen offset:2048 nt lds\n"
"buffer_load_dwordx4 v9, s[64:67], s96 offen offset:3072 nt lds\n"
"s_add_u32 s96, s96, 8192\n"
"s_sub_u32 s94, s94, -8\n"
"s_sub_u32 s92, s92, -8\n"
"s_or_b32 s80, s50, s51\n"
"s_cbranch_scc1 L_noep\n"
"L_ep:\n"
"s_mov_b64 s[82:83], 0\n"
"v_cmp_ne_u64 vcc, s[54:55], v[16:17]\n"
"s_or_b64 s[82:83], s[82:83], vcc\n"
"v_cmp_ne_u64 vcc, s[54:55], v[18:19]\n"
"s_or_b64 s[82:83], s[82:83], vcc\n"
"v_cmp_ne_u64 vcc, s[54:55], v[20:21]\n"
"s_or_b64 s[82:83], s[82:83], vcc\n"
"v_cmp_ne_u64 vcc, s[54:55], v[22:23]\n"
"s_or_b64 s[82:83], s[82:83], vcc\n"
"v_cmp_ne_u64 vcc, s[54:55], v[24:25]\n"
"s_or_b64 s[82:83], s[82:83], vcc\n"
"v_cmp_ne_u64 vcc, s[54:55], v[26:27]\n"
"s_or_b64 s[82:83], s[82:83], vcc\n"
"v_cmp_ne_u64 vcc, s[54:55], v[28:29]\n"
"s_or_b64 s[82:83], s[82:83], vcc\n"
"v_cmp_ne_u64 vcc, s[54:55], v[30:31]\n"
"s_or_b64 s[82:83], s[82:83], vcc\n"
"v_cmp_ne_u64 vcc, s[54:55], v[32:33]\n"
"s_or_b64 s[82:83], s[82:83], vcc\n"
"v_cmp_ne_u64 vcc, s[54:55], v[34:35]\n"
"s_or_b64 s[82:83], s[82:83], vcc\n"
"v_cmp_ne_u64 vcc, s[54:55], v[36:37]\n"
"s_or_b64 s[82:83], s[82:83], vcc\n"
"v_cmp_ne_u64 vcc, s[54:55], v[38:39]\n"
"s_or_b64 s[82:83], s[82:83], vcc\n"
"v_cmp_ne_u64 vcc, s[54:55], v[40:41]\n"
"s_or_b64 s[82:83], s[82:83], vcc\n"
"v_cmp_ne_u64 vcc, s[54:55], v[42:43]\n"
"s_or_b64 s[82:83], s[82:83], vcc\n"
"v_cmp_ne_u64 vcc, s[54:55], v[44:45]\n"
"s_or_b64 s[82:83], s[82:83], vcc\n"
"v_cmp_ne_u64 vcc, s[54:55], v[46:47]\n"
"s_or_b64 s[82:83], s[82:83], vcc\n"
"s_cbranch_scc0 L_epd\n"
"s_sleep 4\n"
"s_mov_b32 s80, 4096\n"
"s_mov_b32 s81, 8192\n"
"buffer_load_dwordx4 v[16:19], v254, s[72:75], s80 offen sc1\n"
"buffer_load_dwordx4 v[20:23], v254, s[72:75], s80 offen offset:1024 sc1\n"
"buffer_load_dwordx4 v[24:27], v254, s[72:75], s80 offen offset:2048 sc1\n"
"buffer_load_dwordx4 v[28:31], v254, s[72:75], s80 offen offset:3072 sc1\n"
"buffer_load_dwordx4 v[32:35], v254, s[72:75], s81 offen sc1\n"
"buffer_load_dwordx4 v[36:39], v254, s[72:75], s81 offen offset:1024 sc1\n"
"buffer_load_dwordx4 v[40:43], v254, s[72:75], s81 offen offset:2048 sc1\n"
"buffer_load_dwordx4 v[44:47], v254, s[72:75], s81 offen offset:3072 sc1\n"
"s_waitcnt vmcnt(0)\n"
"s_sub_u32 s93, s93, 1\n"
"s_cbranch_scc0 L_ep\n"
"s_mov_b32 s93, 0\n"
"L_epd:\n"
"s_mov_b64 exec, 1\n"
"buffer_store_dwordx2 v[250:251], off, s[72:75], 0 sc1\n"
"s_mov_b64 exec, -1\n"
"L_noep:\n"
"s_waitcnt vmcnt(12)\n"
"v_max_u32 v12, 20, v12\n"
"ds_write_b32 v10, v12 offset:256\n"
"s_waitcnt vmcnt(10)\n"
"ds_read_b64_tr_b16 v[98:99], v240 offset:2048\n"
"ds_read_b64_tr_b16 v[102:103], v241 offset:2048\n"
"ds_read_b64_tr_b16 v[106:107], v240 offset:2560\n"
"ds_read_b64_tr_b16 v[110:111], v241 offset:2560\n"
"s_waitcnt lgkmcnt(0)\n"
"s_add_u32 s81, s57, 14336\n"
"s_add_u32 m0, s95, 16384\n"
"s_nop 0\n"
"buffer_load_dwordx4 v0, s[36:39], s81 offen offset:2048 sc1 lds\n"
"buffer_load_dwordx4 v1, s[36:39], s81 offen offset:3072 sc1 lds\n"
"ds_read_b32 v13, v11\n"
"s_waitcnt lgkmcnt(0)\n"
"v_cmp_gt_u32 vcc, 16, v13\n"
"s_cbranch_vccz L_fr8\n"
"s_movk_i32 s80, 16\n"
"s_call_b64 s[98:99], L_spinFR\n"
"L_fr8:\n"
"s_call_b64 s[98:99], L_hpace\n"
"s_add_u32 m0, s95, 32768\n"
"s_nop 0\n"
"buffer_load_dwordx4 v6, s[64:67], s96 offen nt lds\n"
"buffer_load_dwordx4 v7, s[64:67], s96 offen offset:1024 nt lds\n"
"buffer_load_dwordx4 v8, s[64:67], s96 offen offset:2048 nt lds\n"
"buffer_load_dwordx4 v9, s[64:67], s96 offen offset:3072 nt lds\n"
"s_add_u32 s96, s96, 8192\n"
"s_sub_u32 s94, s94, -8\n"
"s_sub_u32 s92, s92, -8\n"
"s_waitcnt vmcnt(12)\n"
"v_max_u32 v12, 21, v12\n"
"ds_write_b32 v10, v12 offset:256\n"
"s_waitcnt vmcnt(10)\n"
"ds_read_b64_tr_b16 v[112:113], v240\n"
"ds_read_b64_tr_b16 v[116:117], v241\n"
"ds_read_b64_tr_b16 v[120:121], v240 offset:512\n"
"ds_read_b64_tr_b16 v[124:125], v241 offset:512\n"
"s_waitcnt lgkmcnt(0)\n"
"s_add_u32 s80, s57, 32768\n"
"s_add_u32 m0, s95, 16384\n"
"s_nop 0\n"
"buffer_load_dwordx4 v0, s[36:39], s80 offen sc1 lds\n"
"buffer_load_dwordx4 v1, s[36:39], s80 offen offset:1024 sc1 lds\n"
"ds_read_b32 v13, v11\n"
"s_waitcnt lgkmcnt(0)\n"
"v_cmp_gt_u32 vcc, 17, v13\n"
"s_cbranch_vccz L_fr9\n"
"s_movk_i32 s80, 17\n"
"s_call_b64 s[98:99], L_spinFR\n"
"L_fr9:\n"
"s_call_b64 s[98:99], L_hpace\n"
"s_add_u32 m0, s95, 147456\n"
"s_nop 0\n"
"buffer_load_dwordx4 v6, s[64:67], s96 offen nt lds\n"
"buffer_load_dwordx4 v7, s[64:67], s96 offen offset:1024 nt lds\n"
"buffer_load_dwordx4 v8, s[64:67], s96 offen offset:2048 nt lds\n"
"buffer_load_dwordx4 v9, s[64:67], s96 offen offset:3072 nt lds\n"
"s_add_u32 s96, s96, 8192\n"
"s_sub_u32 s94, s94, -8\n"
"s_sub_u32 s92, s92, -8\n"
"s_waitcnt vmcnt(12)\n"
"v_max_u32 v12, 22, v12\n"
"ds_write_b32 v10, v12 offset:256\n"
"s_waitcnt vmcnt(10)\n"
"ds_read_b64_tr_b16 v[114:115], v240 offset:2048\n"
"ds_read_b64_tr_b16 v[118:119], v241 offset:2048\n"
"ds_read_b64_tr_b16 v[122:123], v240 offset:2560\n"
"ds_read_b64_tr_b16 v[126:127], v241 offset:2560\n"
"s_waitcnt lgkmcnt(0)\n"
"s_add_u32 s81, s57, 47104\n"
"s_add_u32 m0, s95, 16384\n"
"s_nop 0\n"
"buffer_load_dwordx4 v0, s[36:39], s81 offen offset:2048 sc1 lds\n"
"buffer_load_dwordx4 v1, s[36:39], s81 offen offset:3072 sc1 lds\n"
"ds_read_b32 v13, v11\n"
"s_waitcnt lgkmcnt(0)\n"
"v_cmp_gt_u32 vcc, 18, v13\n"
"s_cbranch_vccz L_fr10\n"
"s_movk_i32 s80, 18\n"
"s_call_b64 s[98:99], L_spinFR\n"
"L_fr10:\n"
"s_call_b64 s[98:99], L_hpace\n"
"s_add_u32 m0, s95, 131072\n"
"s_nop 0\n"
"buffer_load_dwordx4 v6, s[64:67], s96 offen nt lds\n"
"buffer_load_dwordx4 v7, s[64:67], s96 offen offset:1024 nt lds\n"
"buffer_load_dwordx4 v8, s[64:67], s96 offen offset:2048 nt lds\n"
"buffer_load_dwordx4 v9, s[64:67], s96 offen offset:3072 nt lds\n"
"s_add_u32 s96, s96, 8192\n"
"s_sub_u32 s94, s94, -7\n"
"s_sub_u32 s92, s92, -7\n"
"s_waitcnt vmcnt(12)\n"
"v_max_u32 v12, 23, v12\n"
"ds_write_b32 v10, v12 offset:256\n"
"s_waitcnt vmcnt(10)\n"
"ds_read_b64_tr_b16 v[128:129], v240\n"
"ds_read_b64_tr_b16 v[132:133], v241\n"
"ds_read_b64_tr_b16 v[136:137], v240 offset:512\n"
"ds_read_b64_tr_b16 v[140:141], v241 offset:512\n"
"s_waitcnt lgkmcnt(0)\n"
"s_add_u32 s80, s57, 65536\n"
"s_add_u32 m0, s95, 16384\n"
"s_nop 0\n"
"buffer_load_dwordx4 v0, s[36:39], s80 offen sc1 lds\n"
"buffer_load_dwordx4 v1, s[36:39], s80 offen offset:1024 sc1 lds\n"
"ds_read_b32 v13, v11\n"
"s_waitcnt lgkmcnt(0)\n"
"v_cmp_gt_u32 vcc, 19, v13\n"
"s_cbranch_vccz L_fr11\n"
"s_movk_i32 s80, 19\n"
"s_call_b64 s[98:99], L_spinFR\n"
"L_fr11:\n"
"s_call_b64 s[98:99], L_hpace\n"
"s_add_u32 m0, s95, 114688\n"
"s_nop 0\n"
"buffer_load_dwordx4 v6, s[64:67], s96 offen nt lds\n"
"buffer_load_dwordx4 v7, s[64:67], s96 offen offset:1024 nt lds\n"
"buffer_load_dwordx4 v8, s[64:67], s96 offen offset:2048 nt lds\n"
"buffer_load_dwordx4 v9, s[64:67], s96 offen offset:3072 nt lds\n"
"s_add_u32 s96, s96, 8192\n"
"s_sub_u32 s94, s94, -7\n"
"s_sub_u32 s92, s92, -7\n"
"s_waitcnt vmcnt(12)\n"
"v_max_u32 v12, 24, v12\n"
"ds_write_b32 v10, v12 offset:256\n"
"s_waitcnt vmcnt(10)\n"
"ds_read_b64_tr_b16 v[130:131], v240 offset:2048\n"
"ds_read_b64_tr_b16 v[134:135], v241 offset:2048\n"
"ds_read_b64_tr_b16 v[138:139], v240 offset:2560\n"
"ds_read_b64_tr_b16 v[142:143], v241 offset:2560\n"
"s_waitcnt lgkmcnt(0)\n"
"s_add_u32 s81, s57, 79872\n"
"s_add_u32 m0, s95, 16384\n"
"s_nop 0\n"
"buffer_load_dwordx4 v0, s[36:39], s81 offen offset:2048 sc1 lds\n"
"buffer_load_dwordx4 v1, s[36:39], s81 offen offset:3072 sc1 lds\n"
"ds_read_b32 v13, v11\n"
"s_waitcnt lgkmcnt(0)\n"
"v_cmp_gt_u32 vcc, 20, v13\n"
"s_cbranch_vccz L_fr12\n"
"s_movk_i32 s80, 20\n"
"s_call_b64 s[98:99], L_spinFR\n"
"L_fr12:\n"
"s_call_b64 s[98:99], L_hpace\n"
"s_add_u32 m0, s95, 98304\n"
"s_nop 0\n"
"buffer_load_dwordx4 v6, s[64:67], s96 offen nt lds\n"
"buffer_load_dwordx4 v7, s[64:67], s96 offen offset:1024 nt lds\n"
"buffer_load_dwordx4 v8, s[64:67], s96 offen offset:2048 nt lds\n"
"buffer_load_dwordx4 v9, s[64:67], s96 offen offset:3072 nt lds\n"
"s_add_u32 s96, s96, 8192\n"
"s_sub_u32 s94, s94, -6\n"
"s_sub_u32 s92, s92, -6\n"
"s_waitcnt vmcnt(12)\n"
"v_max_u32 v12, 25, v12\n"
"ds_write_b32 v10, v12 offset:256\n"
"s_waitcnt vmcnt(10)\n"
"ds_read_b64_tr_b16 v[144:145], v240\n"
"ds_read_b64_tr_b16 v[148:149], v241\n"
"ds_read_b64_tr_b16 v[152:153], v240 offset:512\n"
"ds_read_b64_tr_b16 v[156:157], v241 offset:512\n"
"s_waitcnt lgkmcnt(0)\n"
"s_add_u32 s80, s57, 98304\n"
"s_add_u32 m0, s95, 16384\n"
"s_nop 0\n"
"buffer_load_dwordx4 v0, s[36:39], s80 offen sc1 lds\n"
"buffer_load_dwordx4 v1, s[36:39], s80 offen offset:1024 sc1 lds\n"
"ds_read_b32 v13, v11\n"
"s_waitcnt lgkmcnt(0)\n"
"v_cmp_gt_u32 vcc, 21, v13\n"
"s_cbranch_vccz L_fr13\n"
"s_movk_i32 s80, 21\n"
"s_call_b64 s[98:99], L_spinFR\n"
"L_fr13:\n"
"s_call_b64 s[98:99], L_hpace\n"
"s_add_u32 m0, s95, 81920\n"
"s_nop 0\n"
"buffer_load_dwordx4 v6, s[64:67], s96 offen nt lds\n"
"buffer_load_dwordx4 v7, s[64:67], s96 offen offset:1024 nt lds\n"
"buffer_load_dwordx4 v8, s[64:67], s96 offen offset:2048 nt lds\n"
"buffer_load_dwordx4 v9, s[64:67], s96 offen offset:3072 nt lds\n"
"s_add_u32 s96, s96, 8192\n"
"s_sub_u32 s94, s94, -5\n"
"s_sub_u32 s92, s92, -5\n"
"s_getpc_b64 s[80:81]\n"
"L_pc2:\n"
"s_lshl2_add_u32 s82, s88, s53\n"
"s_lshl_b32 s82, s82, 8\n"
"v_lshl_add_u32 v246, v254, 2, s82\n"
"v_min_u32 v246, L_exit-L_pc2, v246\n"
"s_mov_b64 exec, 15\n"
"global_load_dword v246, v246, s[80:81]\n"
"s_mov_b64 exec, -1\n"
"s_waitcnt vmcnt(13)\n"
"v_max_u32 v12, 26, v12\n"
"ds_write_b32 v10, v12 offset:256\n"
"s_waitcnt vmcnt(11)\n"
"ds_read_b64_tr_b16 v[146:147], v240 offset:2048\n"
"ds_read_b64_tr_b16 v[150:151], v241 offset:2048\n"
"ds_read_b64_tr_b16 v[154:155], v240 offset:2560\n"
"ds_read_b64_tr_b16 v[158:159], v241 offset:2560\n"
"s_waitcnt lgkmcnt(0)\n"
"s_add_u32 s81, s57, 112640\n"
"s_add_u32 m0, s95, 16384\n"
"s_nop 0\n"
"buffer_load_dwordx4 v0, s[36:39], s81 offen offset:2048 sc1 lds\n"
"buffer_load_dwordx4 v1, s[36:39], s81 offen offset:3072 sc1 lds\n"
"ds_read_b32 v13, v11\n"
"s_waitcnt lgkmcnt(0)\n"
"v_cmp_gt_u32 vcc, 22, v13\n"
"s_cbranch_vccz L_fr14\n"
"s_movk_i32 s80, 22\n"
"s_call_b64 s[98:99], L_spinFR\n"
"L_fr14:\n"
"s_call_b64 s[98:99], L_hpace\n"
"s_add_u32 m0, s95, 65536\n"
"s_nop 0\n"
"buffer_load_dwordx4 v6, s[64:67], s96 offen nt lds\n"
"buffer_load_dwordx4 v7, s[64:67], s96 offen offset:1024 nt lds\n"
"buffer_load_dwordx4 v8, s[64:67], s96 offen offset:2048 nt lds\n"
"buffer_load_dwordx4 v9, s[64:67], s96 offen offset:3072 nt lds\n"
"s_add_u32 s96, s96, 8192\n"
"s_sub_u32 s94, s94, -4\n"
"s_sub_u32 s92, s92, -4\n"
"s_waitcnt vmcnt(13)\n"
"v_max_u32 v12, 27, v12\n"
"ds_write_b32 v10, v12 offset:256\n"
"s_waitcnt vmcnt(11)\n"
"ds_read_b64_tr_b16 v[160:161], v240\n"
"ds_read_b64_tr_b16 v[164:165], v241\n"
"ds_read_b64_tr_b16 v[168:169], v240 offset:512\n"
"ds_read_b64_tr_b16 v[172:173], v241 offset:512\n"
"s_waitcnt lgkmcnt(0)\n"
"s_add_u32 m0, s95, 16384\n"
"s_nop 0\n"
"buffer_load_dwordx4 v0, s[36:39], s58 offen sc1 lds\n"
"buffer_load_dwordx4 v1, s[36:39], s58 offen offset:1024 sc1 lds\n"
"ds_read_b32 v13, v11\n"
"s_waitcnt lgkmcnt(0)\n"
"v_cmp_gt_u32 vcc, 23, v13\n"
"s_cbranch_vccz L_fr15\n"
"s_movk_i32 s80, 23\n"
"s_call_b64 s[98:99], L_spinFR\n"
"L_fr15:\n"
"s_call_b64 s[98:99], L_hpace\n"
"s_add_u32 m0, s95, 49152\n"
"s_nop 0\n"
"buffer_load_dwordx4 v6, s[64:67], s96 offen nt lds\n"
"buffer_load_dwordx4 v7, s[64:67], s96 offen offset:1024 nt lds\n"
"buffer_load_dwordx4 v8, s[64:67], s96 offen offset:2048 nt lds\n"
"buffer_load_dwordx4 v9, s[64:67], s96 offen offset:3072 nt lds\n"
"s_add_u32 s96, s96, 8192\n"
"s_sub_u32 s94, s94, -2\n"
"s_sub_u32 s92, s92, -2\n"
"s_waitcnt vmcnt(13)\n"
"v_max_u32 v12, 28, v12\n"
"ds_write_b32 v10, v12 offset:256\n"
"s_waitcnt vmcnt(10)\n"
"ds_read_b64_tr_b16 v[162:163], v240 offset:2048\n"
"ds_read_b64_tr_b16 v[166:167], v241 offset:2048\n"
"ds_read_b64_tr_b16 v[170:171], v240 offset:2560\n"
"ds_read_b64_tr_b16 v[174:175], v241 offset:2560\n"
"s_waitcnt lgkmcnt(0)\n"
"s_add_u32 s81, s58, 14336\n"
"s_add_u32 m0, s95, 16384\n"
"s_nop 0\n"
"buffer_load_dwordx4 v0, s[36:39], s81 offen offset:2048 sc1 lds\n"
"buffer_load_dwordx4 v1, s[36:39], s81 offen offset:3072 sc1 lds\n"
"ds_read_b32 v13, v11\n"
"s_waitcnt lgkmcnt(0)\n"
"v_cmp_gt_u32 vcc, 24, v13\n"
"s_cbranch_vccz L_fr16\n"
"s_movk_i32 s80, 24\n"
"s_call_b64 s[98:99], L_spinFR\n"
"L_fr16:\n"
"s_call_b64 s[98:99], L_hpace\n"
"s_add_u32 m0, s95, 32768\n"
"s_nop 0\n"
"buffer_load_dwordx4 v6, s[64:67], s96 offen nt lds\n"
"buffer_load_dwordx4 v7, s[64:67], s96 offen offset:1024 nt lds\n"
"buffer_load_dwordx4 v8, s[64:67], s96 offen offset:2048 nt lds\n"
"buffer_load_dwordx4 v9, s[64:67], s96 offen offset:3072 nt lds\n"
"s_add_u32 s96, s96, 8192\n"
"s_sub_u32 s94, s94, -1\n"
"s_sub_u32 s92, s92, -1\n"
"s_waitcnt vmcnt(12)\n"
"v_max_u32 v12, 29, v12\n"
"ds_write_b32 v10, v12 offset:256\n"
"s_waitcnt vmcnt(10)\n"
"ds_read_b64_tr_b16 v[176:177], v240\n"
"ds_read_b64_tr_b16 v[180:181], v241\n"
"ds_read_b64_tr_b16 v[184:185], v240 offset:512\n"
"ds_read_b64_tr_b16 v[188:189], v241 offset:512\n"
"s_waitcnt lgkmcnt(0)\n"
"s_add_u32 s80, s58, 32768\n"
"s_add_u32 m0, s95, 16384\n"
"s_nop 0\n"
"buffer_load_dwordx4 v0, s[36:39], s80 offen sc1 lds\n"
"buffer_load_dwordx4 v1, s[36:39], s80 offen offset:1024 sc1 lds\n"
"ds_read_b32 v13, v11\n"
"s_waitcnt lgkmcnt(0)\n"
"v_cmp_gt_u32 vcc, 25, v13\n"
"s_cbranch_vccz L_fr17\n"
"s_movk_i32 s80, 25\n"
"s_call_b64 s[98:99], L_spinFR\n"
"L_fr17:\n"
"s_call_b64 s[98:99], L_hpace\n"
"s_add_u32 m0, s95, 147456\n"
"s_nop 0\n"
"buffer_load_dwordx4 v6, s[64:67], s96 offen nt lds\n"
"buffer_load_dwordx4 v7, s[64:67], s96 offen offset:1024 nt lds\n"
"buffer_load_dwordx4 v8, s[64:67], s96 offen offset:2048 nt lds\n"
"buffer_load_dwordx4 v9, s[64:67], s96 offen offset:3072 nt lds\n"
"s_add_u32 s96, s96, 8192\n"
"s_sub_u32 s94, s94, 1\n"
"s_sub_u32 s92, s92, 1\n"
"s_waitcnt vmcnt(12)\n"
"v_max_u32 v12, 30, v12\n"
"ds_write_b32 v10, v12 offset:256\n"
"s_waitcnt vmcnt(10)\n"
"ds_read_b64_tr_b16 v[178:179], v240 offset:2048\n"
"ds_read_b64_tr_b16 v[182:183], v241 offset:2048\n"
"ds_read_b64_tr_b16 v[186:187], v240 offset:2560\n"
"ds_read_b64_tr_b16 v[190:191], v241 offset:2560\n"
"s_waitcnt lgkmcnt(0)\n"
"s_add_u32 s81, s58, 47104\n"
"s_add_u32 m0, s95, 16384\n"
"s_nop 0\n"
"buffer_load_dwordx4 v0, s[36:39], s81 offen offset:2048 sc1 lds\n"
"buffer_load_dwordx4 v1, s[36:39], s81 offen offset:3072 sc1 lds\n"
"ds_read_b32 v13, v11\n"
"s_waitcnt lgkmcnt(0)\n"
"v_cmp_gt_u32 vcc, 26, v13\n"
"s_cbranch_vccz L_fr18\n"
"s_movk_i32 s80, 26\n"
"s_call_b64 s[98:99], L_spinFR\n"
"L_fr18:\n"
"s_call_b64 s[98:99], L_hpace\n"
"s_add_u32 m0, s95, 131072\n"
"s_nop 0\n"
"buffer_load_dwordx4 v6, s[64:67], s96 offen nt lds\n"
"buffer_load_dwordx4 v7, s[64:67], s96 offen offset:1024 nt lds\n"
"buffer_load_dwordx4 v8, s[64:67], s96 offen offset:2048 nt lds\n"
"buffer_load_dwordx4 v9, s[64:67], s96 offen offset:3072 nt lds\n"
"s_add_u32 s96, s96, 8192\n"
"s_sub_u32 s94, s94, 3\n"
"s_sub_u32 s92, s92, 3\n"
"s_waitcnt vmcnt(12)\n"
"v_max_u32 v12, 31, v12\n"
"ds_write_b32 v10, v12 offset:256\n"
"s_waitcnt vmcnt(10)\n"
"ds_read_b64_tr_b16 v[192:193], v240\n"
"ds_read_b64_tr_b16 v[196:197], v241\n"
"ds_read_b64_tr_b16 v[200:201], v240 offset:512\n"
"ds_read_b64_tr_b16 v[204:205], v241 offset:512\n"
"s_waitcnt lgkmcnt(0)\n"
"s_add_u32 s80, s58, 65536\n"
"s_add_u32 m0, s95, 16384\n"
"s_nop 0\n"
"buffer_load_dwordx4 v0, s[36:39], s80 offen sc1 lds\n"
"buffer_load_dwordx4 v1, s[36:39], s80 offen offset:1024 sc1 lds\n"
"ds_read_b32 v13, v11\n"
"s_waitcnt lgkmcnt(0)\n"
"v_cmp_gt_u32 vcc, 27, v13\n"
"s_cbranch_vccz L_fr19\n"
"s_movk_i32 s80, 27\n"
"s_call_b64 s[98:99], L_spinFR\n"
"L_fr19:\n"
"s_call_b64 s[98:99], L_hpace\n"
"s_add_u32 m0, s95, 114688\n"
"s_nop 0\n"
"buffer_load_dwordx4 v6, s[64:67], s96 offen nt lds\n"
"buffer_load_dwordx4 v7, s[64:67], s96 offen offset:1024 nt lds\n"
"buffer_load_dwordx4 v8, s[64:67], s96 offen offset:2048 nt lds\n"
"buffer_load_dwordx4 v9, s[64:67], s96 offen offset:3072 nt lds\n"
"s_add_u32 s96, s96, 8192\n"
"s_sub_u32 s94, s94, 6\n"
"s_sub_u32 s92, s92, 6\n"
"s_waitcnt vmcnt(12)\n"
"v_max_u32 v12, 32, v12\n"
"ds_write_b32 v10, v12 offset:256\n"
"s_waitcnt vmcnt(10)\n"
"ds_read_b64_tr_b16 v[194:195], v240 offset:2048\n"
"ds_read_b64_tr_b16 v[198:199], v241 offset:2048\n"
"ds_read_b64_tr_b16 v[202:203], v240 offset:2560\n"
"ds_read_b64_tr_b16 v[206:207], v241 offset:2560\n"
"s_waitcnt lgkmcnt(0)\n"
"s_add_u32 s81, s58, 79872\n"
"s_add_u32 m0, s95, 16384\n"
"s_nop 0\n"
"buffer_load_dwordx4 v0, s[36:39], s81 offen offset:2048 sc1 lds\n"
"buffer_load_dwordx4 v1, s[36:39], s81 offen offset:3072 sc1 lds\n"
"ds_read_b32 v13, v11\n"
"s_waitcnt lgkmcnt(0)\n"
"v_cmp_gt_u32 vcc, 28, v13\n"
"s_cbranch_vccz L_fr20\n"
"s_movk_i32 s80, 28\n"
"s_call_b64 s[98:99], L_spinFR\n"
"L_fr20:\n"
"s_call_b64 s[98:99], L_hpace\n"
"s_add_u32 m0, s95, 98304\n"
"s_nop 0\n"
"buffer_load_dwordx4 v6, s[64:67], s96 offen nt lds\n"
"buffer_load_dwordx4 v7, s[64:67], s96 offen offset:1024 nt lds\n"
"buffer_load_dwordx4 v8, s[64:67], s96 offen offset:2048 nt lds\n"
"buffer_load_dwordx4 v9, s[64:67], s96 offen offset:3072 nt lds\n"
"s_add_u32 s96, s96, 8192\n"
"s_sub_u32 s94, s94, 8\n"
"s_sub_u32 s92, s92, 8\n"
"s_waitcnt vmcnt(12)\n"
"v_max_u32 v12, 33, v12\n"
"ds_write_b32 v10, v12 offset:256\n"
"s_waitcnt vmcnt(10)\n"
"ds_read_b64_tr_b16 v[208:209], v240\n"
"ds_read_b64_tr_b16 v[212:213], v241\n"
"ds_read_b64_tr_b16 v[216:217], v240 offset:512\n"
"ds_read_b64_tr_b16 v[220:221], v241 offset:512\n"
"buffer_load_dwordx4 v[16:19], v0, s[36:39], s59 offen sc1\n"
"buffer_load_dwordx4 v[20:23], v1, s[36:39], s59 offen offset:1024 sc1\n"
"ds_read_b32 v13, v11\n"
"s_waitcnt lgkmcnt(0)\n"
"v_cmp_gt_u32 vcc, 29, v13\n"
"s_cbranch_vccz L_fr21\n"
"s_movk_i32 s80, 29\n"
"s_call_b64 s[98:99], L_spinFR\n"
"L_fr21:\n"
"s_call_b64 s[98:99], L_hpace\n"
"s_add_u32 m0, s95, 81920\n"
"s_nop 0\n"
"buffer_load_dwordx4 v6, s[64:67], s96 offen nt lds\n"
"buffer_load_dwordx4 v7, s[64:67], s96 offen offset:1024 nt lds\n"
"buffer_load_dwordx4 v8, s[64:67], s96 offen offset:2048 nt lds\n"
"buffer_load_dwordx4 v9, s[64:67], s96 offen offset:3072 nt lds\n"
"s_add_u32 s96, s96, 8192\n"
"s_sub_u32 s94, s94, 11\n"
"s_sub_u32 s92, s92, 11\n"
"s_waitcnt vmcnt(12)\n"
"v_max_u32 v12, 34, v12\n"
"ds_write_b32 v10, v12 offset:256\n"
"s_waitcnt vmcnt(10)\n"
"ds_read_b64_tr_b16 v[210:211], v240 offset:2048\n"
"ds_read_b64_tr_b16 v[214:215], v241 offset:2048\n"
"ds_read_b64_tr_b16 v[218:219], v240 offset:2560\n"
"ds_read_b64_tr_b16 v[222:223], v241 offset:2560\n"
"s_add_u32 s81, s59, 14336\n"
"buffer_load_dwordx4 v[24:27], v0, s[36:39], s81 offen offset:2048 sc1\n"
"buffer_load_dwordx4 v[28:31], v1, s[36:39], s81 offen offset:3072 sc1\n"
"ds_read_b32 v13, v11\n"
"s_waitcnt lgkmcnt(0)\n"
"v_cmp_gt_u32 vcc, 30, v13\n"
"s_cbranch_vccz L_fr22\n"
"s_movk_i32 s80, 30\n"
"s_call_b64 s[98:99], L_spinFR\n"
"L_fr22:\n"
"s_call_b64 s[98:99], L_hpace\n"
"s_add_u32 m0, s95, 65536\n"
"s_nop 0\n"
"buffer_load_dwordx4 v6, s[64:67], s96 offen nt lds\n"
"buffer_load_dwordx4 v7, s[64:67], s96 offen offset:1024 nt lds\n"
"buffer_load_dwordx4 v8, s[64:67], s96 offen offset:2048 nt lds\n"
"buffer_load_dwordx4 v9, s[64:67], s96 offen offset:3072 nt lds\n"
"s_add_u32 s96, s96, 8192\n"
"s_sub_u32 s94, s94, 14\n"
"s_sub_u32 s92, s92, 14\n"
"s_waitcnt vmcnt(12)\n"
"v_max_u32 v12, 35, v12\n"
"ds_write_b32 v10, v12 offset:256\n"
"s_add_u32 s80, s59, 32768\n"
"buffer_load_dwordx4 v[32:35], v0, s[36:39], s80 offen sc1\n"
"buffer_load_dwordx4 v[36:39], v1, s[36:39], s80 offen offset:1024 sc1\n"
"ds_read_b32 v13, v11\n"
"s_waitcnt lgkmcnt(0)\n"
"v_cmp_gt_u32 vcc, 31, v13\n"
"s_cbranch_vccz L_fr23\n"
"s_movk_i32 s80, 31\n"
"s_call_b64 s[98:99], L_spinFR\n"
"L_fr23:\n"
"s_call_b64 s[98:99], L_hpace\n"
"s_add_u32 m0, s95, 49152\n"
"s_nop 0\n"
"buffer_load_dwordx4 v6, s[64:67], s96 offen nt lds\n"
"buffer_load_dwordx4 v7, s[64:67], s96 offen offset:1024 nt lds\n"
"buffer_load_dwordx4 v8, s[64:67], s96 offen offset:2048 nt lds\n"
"buffer_load_dwordx4 v9, s[64:67], s96 offen offset:3072 nt lds\n"
"s_add_u32 s96, s96, 8192\n"
"s_sub_u32 s94, s94, 17\n"
"s_sub_u32 s92, s92, 17\n"
"s_lshl2_add_u32 s80, s88, s53\n"
"s_lshl_b32 s80, s80, 14\n"
"s_add_u32 s80, s80, 2097536\n"
"s_bitcmp1_b32 s50, 0\n"
"s_cselect_b32 s80, 0x400000, s80\n"
"v_lshrrev_b32 v246, 5, v254\n"
"v_and_b32 v245, 16, v254\n"
"v_lshlrev_b32 v245, 2, v245\n"
"v_lshl_or_b32 v246, v246, 10, v245\n"
"s_mov_b32 exec_hi, 0\n"
"buffer_load_dword v246, v246, s[68:71], s80 offen\n"
"s_mov_b32 exec_hi, -1\n"
"s_waitcnt vmcnt(13)\n"
"v_max_u32 v12, 36, v12\n"
"ds_write_b32 v10, v12 offset:256\n"
"s_add_u32 s81, s59, 47104\n"
"buffer_load_dwordx4 v[40:43], v0, s[36:39], s81 offen offset:2048 sc1\n"
"buffer_load_dwordx4 v[44:47], v1, s[36:39], s81 offen offset:3072 sc1\n"
"ds_read_b32 v13, v11\n"
"s_waitcnt lgkmcnt(0)\n"
"v_cmp_gt_u32 vcc, 32, v13\n"
"s_cbranch_vccz L_fr24\n"
"s_movk_i32 s80, 32\n"
"s_call_b64 s[98:99], L_spinFR\n"
"L_fr24:\n"
"s_call_b64 s[98:99], L_hpace\n"
"s_add_u32 m0, s95, 32768\n"
"s_nop 0\n"
"buffer_load_dwordx4 v6, s[64:67], s96 offen nt lds\n"
"buffer_load_dwordx4 v7, s[64:67], s96 offen offset:1024 nt lds\n"
"buffer_load_dwordx4 v8, s[64:67], s96 offen offset:2048 nt lds\n"
"buffer_load_dwordx4 v9, s[64:67], s96 offen offset:3072 nt lds\n"
"s_add_u32 s96, s96, 8192\n"
"s_sub_u32 s94, s94, 21\n"
"s_sub_u32 s92, s92, 21\n"
"s_waitcnt vmcnt(13)\n"
"v_max_u32 v12, 37, v12\n"
"ds_write_b32 v10, v12 offset:256\n"
"s_waitcnt lgkmcnt(0)\n"
"s_add_u32 s80, s58, 98304\n"
"s_add_u32 m0, s95, 16384\n"
"s_nop 0\n"
"buffer_load_dwordx4 v0, s[36:39], s80 offen sc1 lds\n"
"buffer_load_dwordx4 v1, s[36:39], s80 offen offset:1024 sc1 lds\n"
"s_call_b64 s[98:99], L_hpace\n"
"s_max_u32 m0, s95, 4096\n"
"s_nop 0\n"
"buffer_load_dwordx4 v6, s[64:67], s96 offen nt lds\n"
"buffer_load_dwordx4 v7, s[64:67], s96 offen offset:1024 nt lds\n"
"buffer_load_dwordx4 v8, s[64:67], s96 offen offset:2048 nt lds\n"
"buffer_load_dwordx4 v9, s[64:67], s96 offen offset:3072 nt lds\n"
"s_add_u32 s96, s96, 8192\n"
"s_waitcnt vmcnt(13)\n"
"v_max_u32 v12, 38, v12\n"
"ds_write_b32 v10, v12 offset:256\n"
"s_waitcnt lgkmcnt(0)\n"
"s_add_u32 s81, s58, 112640\n"
"s_add_u32 m0, s95, 16384\n"
"s_nop 0\n"
"buffer_load_dwordx4 v0, s[36:39], s81 offen offset:2048 sc1 lds\n"
"buffer_load_dwordx4 v1, s[36:39], s81 offen offset:3072 sc1 lds\n"
"ds_read_b32 v13, v11\n"
"s_waitcnt lgkmcnt(0)\n"
"v_cmp_gt_u32 vcc, 33, v13\n"
"s_cbranch_vccz L_fr25\n"
"s_movk_i32 s80, 33\n"
"s_call_b64 s[98:99], L_spinFR\n"
"L_fr25:\n"
"v_lshrrev_b32 v11, 2, v254\n"
"v_add_u32 v11, 256, v11\n"
"s_movk_i32 s80, 38\n"
"s_call_b64 s[98:99], L_spinFR0\n"
"v_lshrrev_b32 v11, 8, v254\n"
"v_lshlrev_b32 v11, 9, v11\n"
"s_xor_b32 s80, s56, 0x200000\n"
"s_add_u32 s81, s80, 14336\n"
"s_add_u32 m0, s95, 147456\n"
"s_nop 0\n"
"buffer_load_dwordx4 v0, s[68:71], s80 offen lds\n"
"buffer_load_dwordx4 v1, s[68:71], s80 offen offset:1024 lds\n"
"buffer_load_dwordx4 v0, s[68:71], s81 offen offset:2048 lds\n"
"buffer_load_dwordx4 v1, s[68:71], s81 offen offset:3072 lds\n"
"ds_read_b32 v13, v11\n"
"s_waitcnt vmcnt(12)\n"
"v_max_u32 v12, 39, v12\n"
"ds_write_b32 v10, v12 offset:256\n"
"s_waitcnt lgkmcnt(1)\n"
"v_cmp_gt_u32 vcc, 34, v13\n"
"s_cbranch_vccz L_fr26\n"
"s_movk_i32 s80, 34\n"
"s_call_b64 s[98:99], L_spinFR\n"
"L_fr26:\n"
"s_xor_b32 s80, s56, 0x200000\n"
"s_add_u32 s81, s80, 47104\n"
"s_add_u32 s80, s80, 32768\n"
"s_add_u32 m0, s95, 131072\n"
"s_nop 0\n"
"buffer_load_dwordx4 v0, s[68:71], s80 offen lds\n"
"buffer_load_dwordx4 v1, s[68:71], s80 offen offset:1024 lds\n"
"buffer_load_dwordx4 v0, s[68:71], s81 offen offset:2048 lds\n"
"buffer_load_dwordx4 v1, s[68:71], s81 offen offset:3072 lds\n"
"ds_read_b32 v13, v11\n"
"s_waitcnt vmcnt(10)\n"
"v_max_u32 v12, 40, v12\n"
"ds_write_b32 v10, v12 offset:256\n"
"s_waitcnt lgkmcnt(1)\n"
"v_cmp_gt_u32 vcc, 35, v13\n"
"s_cbranch_vccz L_fr27\n"
"s_movk_i32 s80, 35\n"
"s_call_b64 s[98:99], L_spinFR\n"
"L_fr27:\n"
"s_xor_b32 s80, s56, 0x200000\n"
"s_add_u32 s81, s80, 79872\n"
"s_add_u32 s80, s80, 65536\n"
"s_add_u32 m0, s95, 114688\n"
"s_nop 0\n"
"buffer_load_dwordx4 v0, s[68:71], s80 offen lds\n"
"buffer_load_dwordx4 v1, s[68:71], s80 offen offset:1024 lds\n"
"buffer_load_dwordx4 v0, s[68:71], s81 offen offset:2048 lds\n"
"buffer_load_dwordx4 v1, s[68:71], s81 offen offset:3072 lds\n"
"ds_read_b32 v13, v11\n"
"s_waitcnt vmcnt(8)\n"
"s_waitcnt lgkmcnt(0)\n"
"v_cmp_gt_u32 vcc, 36, v13\n"
"s_cbranch_vccz L_fr28\n"
"s_movk_i32 s80, 36\n"
"s_call_b64 s[98:99], L_spinFR\n"
"L_fr28:\n"
"s_xor_b32 s80, s56, 0x200000\n"
"s_add_u32 s81, s80, 112640\n"
"s_add_u32 s80, s80, 98304\n"
"s_add_u32 m0, s95, 98304\n"
"s_nop 0\n"
"buffer_load_dwordx4 v0, s[68:71], s80 offen lds\n"
"buffer_load_dwordx4 v1, s[68:71], s80 offen offset:1024 lds\n"
"buffer_load_dwordx4 v0, s[68:71], s81 offen offset:2048 lds\n"
"buffer_load_dwordx4 v1, s[68:71], s81 offen offset:3072 lds\n"
"ds_read_b32 v13, v11\n"
"s_waitcnt vmcnt(8)\n"
"s_waitcnt lgkmcnt(0)\n"
"v_cmp_gt_u32 vcc, 37, v13\n"
"s_cbranch_vccz L_fr29\n"
"s_movk_i32 s80, 37\n"
"s_call_b64 s[98:99], L_spinFR\n"
"L_fr29:\n"
"s_xor_b32 s80, s57, 0x200000\n"
"s_add_u32 s81, s80, 14336\n"
"s_add_u32 m0, s95, 81920\n"
"s_nop 0\n"
"buffer_load_dwordx4 v0, s[68:71], s80 offen lds\n"
"buffer_load_dwordx4 v1, s[68:71], s80 offen offset:1024 lds\n"
"buffer_load_dwordx4 v0, s[68:71], s81 offen offset:2048 lds\n"
"buffer_load_dwordx4 v1, s[68:71], s81 offen offset:3072 lds\n"
"s_waitcnt vmcnt(8)\n"
"s_waitcnt vmcnt(4)\n"
"ds_read_b32 v13, v11\n"
"s_waitcnt vmcnt(0)\n"
"v_max_u32 v12, 41, v12\n"
"ds_write_b32 v10, v12 offset:256\n"
"s_waitcnt lgkmcnt(1)\n"
"v_cmp_gt_u32 vcc, 40, v13\n"
"s_cbranch_vccz L_fr30\n"
"s_movk_i32 s80, 40\n"
"s_call_b64 s[98:99], L_spinFR\n"
"L_fr30:\n"
"v_mov_b32 v250, v0\n"
"v_mov_b32 v251, v1\n"
"v_xor_b32 v252, 32, v255\n"
"ds_read_b32 v13, v11\n"
"s_waitcnt lgkmcnt(0)\n"
"v_cmp_gt_u32 vcc, 41, v13\n"
"s_cbranch_vccz L_gate\n"
"s_movk_i32 s80, 41\n"
"s_mov_b32 s81, s93\n"
"s_movk_i32 s93, 200\n"
"s_call_b64 s[98:99], L_spinFR\n"
"s_mov_b32 s93, s81\n"
"L_gate:\n"
"ds_write_b128 v15, v[16:19]\n"
"ds_write_b128 v15, v[20:23] offset:1024\n"
"ds_write_b128 v15, v[24:27] offset:2048\n"
"ds_write_b128 v15, v[28:31] offset:3072\n"
"ds_write_b128 v15, v[32:35] offset:16384\n"
"ds_write_b128 v15, v[36:39] offset:17408\n"
"ds_write_b128 v15, v[40:43] offset:18432\n"
"ds_write_b128 v15, v[44:47] offset:19456\n"
"ds_write_b128 v15, v[224:227] offset:32768\n"
"ds_write_b128 v15, v[228:231] offset:33792\n"
"ds_write_b128 v15, v[232:235] offset:34816\n"
"ds_write_b128 v15, v[236:239] offset:35840\n"
"s_setprio 0\n"
"L_hp0:\n"
"buffer_load_dwordx2 v[248:249], v253, s[72:75], 0 offen sc1\n"
"s_waitcnt vmcnt(0)\n"
"v_cmp_eq_u64 vcc, s[54:55], v[248:249]\n"
"s_mov_b32 s94, vcc_lo\n"
"s_bitcmp1_b32 s94, 0\n"
"s_cbranch_scc1 L_hd0\n"
"s_sleep 2\n"
"s_sub_u32 s93, s93, 1\n"
"s_cbranch_scc0 L_hp0\n"
"s_mov_b32 s93, 0\n"
"L_hd0:\n"
"buffer_load_dwordx4 v[224:227], v254, s[72:75], s60 offen sc1\n"
"buffer_load_dwordx4 v[228:231], v254, s[72:75], s60 offen offset:1024 sc1\n"
"buffer_load_dwordx4 v[232:235], v254, s[72:75], s60 offen offset:2048 sc1\n"
"s_add_u32 s60, s60, 3072\n"
"buffer_load_dwordx4 v[236:239], v254, s[72:75], s60 offen sc1\n"
"buffer_load_dwordx4 v[240:243], v254, s[72:75], s60 offen offset:1024 sc1\n"
"buffer_load_dwordx4 v[244:247], v254, s[72:75], s60 offen offset:2048 sc1\n"
"s_waitcnt vmcnt(5)\n"
"v_mfma_f32_16x16x32_bf16 v[0:3], v[224:227], v[48:51], 0\n"
"v_mfma_f32_16x16x32_bf16 v[12:15], v[224:227], v[52:55], 0\n"
"v_mfma_f32_16x16x32_bf16 v[24:27], v[224:227], v[56:59], 0\n"
"v_mfma_f32_16x16x32_bf16 v[36:39], v[224:227], v[60:63], 0\n"
"s_bitcmp1_b32 s94, 1\n"
"s_cbranch_scc1 L_hr1\n"
"buffer_load_dwordx2 v[248:249], v253, s[72:75], 0 offen sc1\n"
"L_hr1:\n"
"s_add_u32 s60, s60, 3072\n"
"buffer_load_dwordx4 v[224:227], v254, s[72:75], s60 offen sc1\n"
"s_waitcnt vmcnt(5)\n"
"v_mfma_f32_16x16x32_bf16 v[4:7], v[228:231], v[48:51], 0\n"
"v_mfma_f32_16x16x32_bf16 v[16:19], v[228:231], v[52:55], 0\n"
"v_mfma_f32_16x16x32_bf16 v[28:31], v[228:231], v[56:59], 0\n"
"v_mfma_f32_16x16x32_bf16 v[40:43], v[228:231], v[60:63], 0\n"
"buffer_load_dwordx4 v[228:231], v254, s[72:75], s60 offen offset:1024 sc1\n"
"s_waitcnt vmcnt(5)\n"
"v_mfma_f32_16x16x32_bf16 v[8:11], v[232:235], v[48:51], 0\n"
"v_mfma_f32_16x16x32_bf16 v[20:23], v[232:235], v[52:55], 0\n"
"v_mfma_f32_16x16x32_bf16 v[32:35], v[232:235], v[56:59], 0\n"
"v_mfma_f32_16x16x32_bf16 v[44:47], v[232:235], v[60:63], 0\n"
"buffer_load_dwordx4 v[232:235], v254, s[72:75], s60 offen offset:2048 sc1\n"
"ds_read_b64_tr_b16 v[48:49], v255\n"
"ds_read_b64_tr_b16 v[50:51], v255 offset:2048\n"
"ds_read_b64_tr_b16 v[52:53], v252\n"
"ds_read_b64_tr_b16 v[54:55], v252 offset:2048\n"
"ds_read_b64_tr_b16 v[56:57], v255 offset:512\n"
"ds_read_b64_tr_b16 v[58:59], v255 offset:2560\n"
"ds_read_b64_tr_b16 v[60:61], v252 offset:512\n"
"ds_read_b64_tr_b16 v[62:63], v252 offset:2560\n"
"s_waitcnt vmcnt(5)\n"
"v_mfma_f32_16x16x32_bf16 v[0:3], v[236:239], v[64:67], v[0:3]\n"
"v_mfma_f32_16x16x32_bf16 v[12:15], v[236:239], v[68:71], v[12:15]\n"
"v_mfma_f32_16x16x32_bf16 v[24:27], v[236:239], v[72:75], v[24:27]\n"
"v_mfma_f32_16x16x32_bf16 v[36:39], v[236:239], v[76:79], v[36:39]\n"
"s_add_u32 s60, s60, 3072\n"
"buffer_load_dwordx4 v[236:239], v254, s[72:75], s60 offen sc1\n"
"s_waitcnt vmcnt(5)\n"
"v_mfma_f32_16x16x32_bf16 v[4:7], v[240:243], v[64:67], v[4:7]\n"
"v_mfma_f32_16x16x32_bf16 v[16:19], v[240:243], v[68:71], v[16:19]\n"
"v_mfma_f32_16x16x32_bf16 v[28:31], v[240:243], v[72:75], v[28:31]\n"
"v_mfma_f32_16x16x32_bf16 v[40:43], v[240:243], v[76:79], v[40:43]\n"
"buffer_load_dwordx4 v[240:243], v254, s[72:75], s60 offen offset:1024 sc1\n"
"s_waitcnt vmcnt(5)\n"
"v_mfma_f32_16x16x32_bf16 v[8:11], v[244:247], v[64:67], v[8:11]\n"
"v_mfma_f32_16x16x32_bf16 v[20:23], v[244:247], v[68:71], v[20:23]\n"
"v_mfma_f32_16x16x32_bf16 v[32:35], v[244:247], v[72:75], v[32:35]\n"
"v_mfma_f32_16x16x32_bf16 v[44:47], v[244:247], v[76:79], v[44:47]\n"
"buffer_load_dwordx4 v[244:247], v254, s[72:75], s60 offen offset:2048 sc1\n"
"s_waitcnt lgkmcnt(0)\n"
"s_add_u32 s81, s59, 112640\n"
"s_add_u32 s80, s59, 98304\n"
"s_add_u32 m0, s95, 16384\n"
"s_nop 0\n"
"buffer_load_dwordx4 v250, s[36:39], s80 offen sc1 lds\n"
"buffer_load_dwordx4 v251, s[36:39], s80 offen offset:1024 sc1 lds\n"
"buffer_load_dwordx4 v250, s[36:39], s81 offen offset:2048 sc1 lds\n"
"buffer_load_dwordx4 v251, s[36:39], s81 offen offset:3072 sc1 lds\n"
"ds_read_b64_tr_b16 v[64:65], v255 offset:16384\n"
"ds_read_b64_tr_b16 v[66:67], v255 offset:18432\n"
"ds_read_b64_tr_b16 v[68:69], v252 offset:16384\n"
"ds_read_b64_tr_b16 v[70:71], v252 offset:18432\n"
"ds_read_b64_tr_b16 v[72:73], v255 offset:16896\n"
"ds_read_b64_tr_b16 v[74:75], v255 offset:18944\n"
"ds_read_b64_tr_b16 v[76:77], v252 offset:16896\n"
"ds_read_b64_tr_b16 v[78:79], v252 offset:18944\n"
"s_waitcnt vmcnt(9)\n"
"v_mfma_f32_16x16x32_bf16 v[0:3], v[224:227], v[80:83], v[0:3]\n"
"v_mfma_f32_16x16x32_bf16 v[12:15], v[224:227], v[84:87], v[12:15]\n"
"v_mfma_f32_16x16x32_bf16 v[24:27], v[224:227], v[88:91], v[24:27]\n"
"v_mfma_f32_16x16x32_bf16 v[36:39], v[224:227], v[92:95], v[36:39]\n"
"v_cmp_eq_u64 vcc, s[54:55], v[248:249]\n"
"s_mov_b32 s94, vcc_lo\n"
"s_bitcmp1_b32 s94, 1\n"
"s_cbranch_scc1 L_hd1\n"
"L_hp1:\n"
"buffer_load_dwordx2 v[248:249], v253, s[72:75], 0 offen sc1\n"
"s_waitcnt vmcnt(0)\n"
"v_cmp_eq_u64 vcc, s[54:55], v[248:249]\n"
"s_mov_b32 s94, vcc_lo\n"
"s_bitcmp1_b32 s94, 1\n"
"s_cbranch_scc1 L_hd1\n"
"s_sleep 2\n"
"s_sub_u32 s93, s93, 1\n"
"s_cbranch_scc0 L_hp1\n"
"s_mov_b32 s93, 0\n"
"L_hd1:\n"
"buffer_load_dwordx4 v[224:227], v254, s[72:75], s61 offen sc1\n"
"s_waitcnt vmcnt(9)\n"
"v_mfma_f32_16x16x32_bf16 v[4:7], v[228:231], v[80:83], v[4:7]\n"
"v_mfma_f32_16x16x32_bf16 v[16:19], v[228:231], v[84:87], v[16:19]\n"
"v_mfma_f32_16x16x32_bf16 v[28:31], v[228:231], v[88:91], v[28:31]\n"
"v_mfma_f32_16x16x32_bf16 v[40:43], v[228:231], v[92:95], v[40:43]\n"
"buffer_load_dwordx4 v[228:231], v254, s[72:75], s61 offen offset:1024 sc1\n"
"s_waitcnt vmcnt(9)\n"
"v_mfma_f32_16x16x32_bf16 v[8:11], v[232:235], v[80:83], v[8:11]\n"
"v_mfma_f32_16x16x32_bf16 v[20:23], v[232:235], v[84:87], v[20:23]\n"
"v_mfma_f32_16x16x32_bf16 v[32:35], v[232:235], v[88:91], v[32:35]\n"
"v_mfma_f32_16x16x32_bf16 v[44:47], v[232:235], v[92:95], v[44:47]\n"
"buffer_load_dwordx4 v[232:235], v254, s[72:75], s61 offen offset:2048 sc1\n"
"ds_read_b64_tr_b16 v[80:81], v255 offset:32768\n"
"ds_read_b64_tr_b16 v[82:83], v255 offset:34816\n"
"ds_read_b64_tr_b16 v[84:85], v252 offset:32768\n"
"ds_read_b64_tr_b16 v[86:87], v252 offset:34816\n"
"ds_read_b64_tr_b16 v[88:89], v255 offset:33280\n"
"ds_read_b64_tr_b16 v[90:91], v255 offset:35328\n"
"ds_read_b64_tr_b16 v[92:93], v252 offset:33280\n"
"ds_read_b64_tr_b16 v[94:95], v252 offset:35328\n"
"s_waitcnt vmcnt(9)\n"
"v_mfma_f32_16x16x32_bf16 v[0:3], v[236:239], v[96:99], v[0:3]\n"
"v_mfma_f32_16x16x32_bf16 v[12:15], v[236:239], v[100:103], v[12:15]\n"
"v_mfma_f32_16x16x32_bf16 v[24:27], v[236:239], v[104:107], v[24:27]\n"
"v_mfma_f32_16x16x32_bf16 v[36:39], v[236:239], v[108:111], v[36:39]\n"
"s_add_u32 s61, s61, 3072\n"
"buffer_load_dwordx4 v[236:239], v254, s[72:75], s61 offen sc1\n"
"s_waitcnt vmcnt(9)\n"
"v_mfma_f32_16x16x32_bf16 v[4:7], v[240:243], v[96:99], v[4:7]\n"
"v_mfma_f32_16x16x32_bf16 v[16:19], v[240:243], v[100:103], v[16:19]\n"
"v_mfma_f32_16x16x32_bf16 v[28:31], v[240:243], v[104:107], v[28:31]\n"
"v_mfma_f32_16x16x32_bf16 v[40:43], v[240:243], v[108:111], v[40:43]\n"
"buffer_load_dwordx4 v[240:243], v254, s[72:75], s61 offen offset:1024 sc1\n"
"s_waitcnt vmcnt(9)\n"
"v_mfma_f32_16x16x32_bf16 v[8:11], v[244:247], v[96:99], v[8:11]\n"
"v_mfma_f32_16x16x32_bf16 v[20:23], v[244:247], v[100:103], v[20:23]\n"
"v_mfma_f32_16x16x32_bf16 v[32:35], v[244:247], v[104:107], v[32:35]\n"
"v_mfma_f32_16x16x32_bf16 v[44:47], v[244:247], v[108:111], v[44:47]\n"
"buffer_load_dwordx4 v[244:247], v254, s[72:75], s61 offen offset:2048 sc1\n"
"ds_read_b64_tr_b16 v[96:97], v255 offset:49152\n"
"ds_read_b64_tr_b16 v[98:99], v255 offset:51200\n"
"ds_read_b64_tr_b16 v[100:101], v252 offset:49152\n"
"ds_read_b64_tr_b16 v[102:103], v252 offset:51200\n"
"ds_read_b64_tr_b16 v[104:105], v255 offset:49664\n"
"ds_read_b64_tr_b16 v[106:107], v255 offset:51712\n"
"ds_read_b64_tr_b16 v[108:109], v252 offset:49664\n"
"ds_read_b64_tr_b16 v[110:111], v252 offset:51712\n"
"s_waitcnt vmcnt(5)\n"
"v_mfma_f32_16x16x32_bf16 v[0:3], v[224:227], v[112:115], v[0:3]\n"
"v_mfma_f32_16x16x32_bf16 v[12:15], v[224:227], v[116:119], v[12:15]\n"
"v_mfma_f32_16x16x32_bf16 v[24:27], v[224:227], v[120:123], v[24:27]\n"
"v_mfma_f32_16x16x32_bf16 v[36:39], v[224:227], v[124:127], v[36:39]\n"
"s_bitcmp1_b32 s94, 2\n"
"s_cbranch_scc1 L_hr2\n"
"buffer_load_dwordx2 v[248:249], v253, s[72:75], 0 offen sc1\n"
"L_hr2:\n"
"s_add_u32 s61, s61, 3072\n"
"buffer_load_dwordx4 v[224:227], v254, s[72:75], s61 offen sc1\n"
"s_waitcnt vmcnt(5)\n"
"v_mfma_f32_16x16x32_bf16 v[4:7], v[228:231], v[112:115], v[4:7]\n"
"v_mfma_f32_16x16x32_bf16 v[16:19], v[228:231], v[116:119], v[16:19]\n"
"v_mfma_f32_16x16x32_bf16 v[28:31], v[228:231], v[120:123], v[28:31]\n"
"v_mfma_f32_16x16x32_bf16 v[40:43], v[228:231], v[124:127], v[40:43]\n"
"buffer_load_dwordx4 v[228:231], v254, s[72:75], s61 offen offset:1024 sc1\n"
"s_waitcnt vmcnt(5)\n"
"v_mfma_f32_16x16x32_bf16 v[8:11], v[232:235], v[112:115], v[8:11]\n"
"v_mfma_f32_16x16x32_bf16 v[20:23], v[232:235], v[116:119], v[20:23]\n"
"v_mfma_f32_16x16x32_bf16 v[32:35], v[232:235], v[120:123], v[32:35]\n"
"v_mfma_f32_16x16x32_bf16 v[44:47], v[232:235], v[124:127], v[44:47]\n"
"buffer_load_dwordx4 v[232:235], v254, s[72:75], s61 offen offset:2048 sc1\n"
"s_waitcnt vmcnt(5)\n"
"v_mfma_f32_16x16x32_bf16 v[0:3], v[236:239], v[128:131], v[0:3]\n"
"v_mfma_f32_16x16x32_bf16 v[12:15], v[236:239], v[132:135], v[12:15]\n"
"v_mfma_f32_16x16x32_bf16 v[24:27], v[236:239], v[136:139], v[24:27]\n"
"v_mfma_f32_16x16x32_bf16 v[36:39], v[236:239], v[140:143], v[36:39]\n"
"s_add_u32 s61, s61, 3072\n"
"buffer_load_dwordx4 v[236:239], v254, s[72:75], s61 offen sc1\n"
"s_waitcnt vmcnt(5)\n"
"v_mfma_f32_16x16x32_bf16 v[4:7], v[240:243], v[128:131], v[4:7]\n"
"v_mfma_f32_16x16x32_bf16 v[16:19], v[240:243], v[132:135], v[16:19]\n"
"v_mfma_f32_16x16x32_bf16 v[28:31], v[240:243], v[136:139], v[28:31]\n"
"v_mfma_f32_16x16x32_bf16 v[40:43], v[240:243], v[140:143], v[40:43]\n"
"buffer_load_dwordx4 v[240:243], v254, s[72:75], s61 offen offset:1024 sc1\n"
"s_waitcnt vmcnt(5)\n"
"v_mfma_f32_16x16x32_bf16 v[8:11], v[244:247], v[128:131], v[8:11]\n"
"v_mfma_f32_16x16x32_bf16 v[20:23], v[244:247], v[132:135], v[20:23]\n"
"v_mfma_f32_16x16x32_bf16 v[32:35], v[244:247], v[136:139], v[32:35]\n"
"v_mfma_f32_16x16x32_bf16 v[44:47], v[244:247], v[140:143], v[44:47]\n"
"buffer_load_dwordx4 v[244:247], v254, s[72:75], s61 offen offset:2048 sc1\n"
"ds_read_b64_tr_b16 v[112:113], v255\n"
"ds_read_b64_tr_b16 v[114:115], v255 offset:2048\n"
"ds_read_b64_tr_b16 v[116:117], v252\n"
"ds_read_b64_tr_b16 v[118:119], v252 offset:2048\n"
"ds_read_b64_tr_b16 v[120:121], v255 offset:512\n"
"ds_read_b64_tr_b16 v[122:123], v255 offset:2560\n"
"ds_read_b64_tr_b16 v[124:125], v252 offset:512\n"
"ds_read_b64_tr_b16 v[126:127], v252 offset:2560\n"
"s_waitcnt vmcnt(5)\n"
"v_mfma_f32_16x16x32_bf16 v[0:3], v[224:227], v[144:147], v[0:3]\n"
"v_mfma_f32_16x16x32_bf16 v[12:15], v[224:227], v[148:151], v[12:15]\n"
"v_mfma_f32_16x16x32_bf16 v[24:27], v[224:227], v[152:155], v[24:27]\n"
"v_mfma_f32_16x16x32_bf16 v[36:39], v[224:227], v[156:159], v[36:39]\n"
"v_cmp_eq_u64 vcc, s[54:55], v[248:249]\n"
"s_mov_b32 s94, vcc_lo\n"
"s_bitcmp1_b32 s94, 2\n"
"s_cbranch_scc1 L_hd2\n"
"L_hp2:\n"
"buffer_load_dwordx2 v[248:249], v253, s[72:75], 0 offen sc1\n"
"s_waitcnt vmcnt(0)\n"
"v_cmp_eq_u64 vcc, s[54:55], v[248:249]\n"
"s_mov_b32 s94, vcc_lo\n"
"s_bitcmp1_b32 s94, 2\n"
"s_cbranch_scc1 L_hd2\n"
"s_sleep 2\n"
"s_sub_u32 s93, s93, 1\n"
"s_cbranch_scc0 L_hp2\n"
"s_mov_b32 s93, 0\n"
"L_hd2:\n"
"buffer_load_dwordx4 v[224:227], v254, s[72:75], s62 offen sc1\n"
"s_waitcnt vmcnt(5)\n"
"v_mfma_f32_16x16x32_bf16 v[4:7], v[228:231], v[144:147], v[4:7]\n"
"v_mfma_f32_16x16x32_bf16 v[16:19], v[228:231], v[148:151], v[16:19]\n"
"v_mfma_f32_16x16x32_bf16 v[28:31], v[228:231], v[152:155], v[28:31]\n"
"v_mfma_f32_16x16x32_bf16 v[40:43], v[228:231], v[156:159], v[40:43]\n"
"buffer_load_dwordx4 v[228:231], v254, s[72:75], s62 offen offset:1024 sc1\n"
"s_waitcnt vmcnt(5)\n"
"v_mfma_f32_16x16x32_bf16 v[8:11], v[232:235], v[144:147], v[8:11]\n"
"v_mfma_f32_16x16x32_bf16 v[20:23], v[232:235], v[148:151], v[20:23]\n"
"v_mfma_f32_16x16x32_bf16 v[32:35], v[232:235], v[152:155], v[32:35]\n"
"v_mfma_f32_16x16x32_bf16 v[44:47], v[232:235], v[156:159], v[44:47]\n"
"buffer_load_dwordx4 v[232:235], v254, s[72:75], s62 offen offset:2048 sc1\n"
"s_waitcnt vmcnt(5)\n"
"v_mfma_f32_16x16x32_bf16 v[0:3], v[236:239], v[160:163], v[0:3]\n"
"v_mfma_f32_16x16x32_bf16 v[12:15], v[236:239], v[164:167], v[12:15]\n"
"v_mfma_f32_16x16x32_bf16 v[24:27], v[236:239], v[168:171], v[24:27]\n"
"v_mfma_f32_16x16x32_bf16 v[36:39], v[236:239], v[172:175], v[36:39]\n"
"s_add_u32 s62, s62, 3072\n"
"buffer_load_dwordx4 v[236:239], v254, s[72:75], s62 offen sc1\n"
"s_waitcnt vmcnt(5)\n"
"v_mfma_f32_16x16x32_bf16 v[4:7], v[240:243], v[160:163], v[4:7]\n"
"v_mfma_f32_16x16x32_bf16 v[16:19], v[240:243], v[164:167], v[16:19]\n"
"v_mfma_f32_16x16x32_bf16 v[28:31], v[240:243], v[168:171], v[28:31]\n"
"v_mfma_f32_16x16x32_bf16 v[40:43], v[240:243], v[172:175], v[40:43]\n"
"buffer_load_dwordx4 v[240:243], v254, s[72:75], s62 offen offset:1024 sc1\n"
"s_waitcnt vmcnt(5)\n"
"v_mfma_f32_16x16x32_bf16 v[8:11], v[244:247], v[160:163], v[8:11]\n"
"v_mfma_f32_16x16x32_bf16 v[20:23], v[244:247], v[164:167], v[20:23]\n"
"v_mfma_f32_16x16x32_bf16 v[32:35], v[244:247], v[168:171], v[32:35]\n"
"v_mfma_f32_16x16x32_bf16 v[44:47], v[244:247], v[172:175], v[44:47]\n"
"buffer_load_dwordx4 v[244:247], v254, s[72:75], s62 offen offset:2048 sc1\n"
"s_waitcnt vmcnt(5)\n"
"v_mfma_f32_16x16x32_bf16 v[0:3], v[224:227], v[176:179], v[0:3]\n"
"v_mfma_f32_16x16x32_bf16 v[12:15], v[224:227], v[180:183], v[12:15]\n"
"v_mfma_f32_16x16x32_bf16 v[24:27], v[224:227], v[184:187], v[24:27]\n"
"v_mfma_f32_16x16x32_bf16 v[36:39], v[224:227], v[188:191], v[36:39]\n"
"s_bitcmp1_b32 s94, 3\n"
"s_cbranch_scc1 L_hr3\n"
"buffer_load_dwordx2 v[248:249], v253, s[72:75], 0 offen sc1\n"
"L_hr3:\n"
"s_add_u32 s62, s62, 3072\n"
"buffer_load_dwordx4 v[224:227], v254, s[72:75], s62 offen sc1\n"
"s_waitcnt vmcnt(5)\n"
"v_mfma_f32_16x16x32_bf16 v[4:7], v[228:231], v[176:179], v[4:7]\n"
"v_mfma_f32_16x16x32_bf16 v[16:19], v[228:231], v[180:183], v[16:19]\n"
"v_mfma_f32_16x16x32_bf16 v[28:31], v[228:231], v[184:187], v[28:31]\n"
"v_mfma_f32_16x16x32_bf16 v[40:43], v[228:231], v[188:191], v[40:43]\n"
"buffer_load_dwordx4 v[228:231], v254, s[72:75], s62 offen offset:1024 sc1\n"
"s_waitcnt vmcnt(5)\n"
"v_mfma_f32_16x16x32_bf16 v[8:11], v[232:235], v[176:179], v[8:11]\n"
"v_mfma_f32_16x16x32_bf16 v[20:23], v[232:235], v[180:183], v[20:23]\n"
"v_mfma_f32_16x16x32_bf16 v[32:35], v[232:235], v[184:187], v[32:35]\n"
"v_mfma_f32_16x16x32_bf16 v[44:47], v[232:235], v[188:191], v[44:47]\n"
"buffer_load_dwordx4 v[232:235], v254, s[72:75], s62 offen offset:2048 sc1\n"
"s_waitcnt vmcnt(5)\n"
"v_mfma_f32_16x16x32_bf16 v[0:3], v[236:239], v[192:195], v[0:3]\n"
"v_mfma_f32_16x16x32_bf16 v[12:15], v[236:239], v[196:199], v[12:15]\n"
"v_mfma_f32_16x16x32_bf16 v[24:27], v[236:239], v[200:203], v[24:27]\n"
"v_mfma_f32_16x16x32_bf16 v[36:39], v[236:239], v[204:207], v[36:39]\n"
"s_add_u32 s62, s62, 3072\n"
"buffer_load_dwordx4 v[236:239], v254, s[72:75], s62 offen sc1\n"
"s_waitcnt vmcnt(5)\n"
"v_mfma_f32_16x16x32_bf16 v[4:7], v[240:243], v[192:195], v[4:7]\n"
"v_mfma_f32_16x16x32_bf16 v[16:19], v[240:243], v[196:199], v[16:19]\n"
"v_mfma_f32_16x16x32_bf16 v[28:31], v[240:243], v[200:203], v[28:31]\n"
"v_mfma_f32_16x16x32_bf16 v[40:43], v[240:243], v[204:207], v[40:43]\n"
"buffer_load_dwordx4 v[240:243], v254, s[72:75], s62 offen offset:1024 sc1\n"
"s_waitcnt vmcnt(5)\n"
"v_mfma_f32_16x16x32_bf16 v[8:11], v[244:247], v[192:195], v[8:11]\n"
"v_mfma_f32_16x16x32_bf16 v[20:23], v[244:247], v[196:199], v[20:23]\n"
"v_mfma_f32_16x16x32_bf16 v[32:35], v[244:247], v[200:203], v[32:35]\n"
"v_mfma_f32_16x16x32_bf16 v[44:47], v[244:247], v[204:207], v[44:47]\n"
"buffer_load_dwordx4 v[244:247], v254, s[72:75], s62 offen offset:2048 sc1\n"
"s_waitcnt vmcnt(5)\n"
"v_mfma_f32_16x16x32_bf16 v[0:3], v[224:227], v[208:211], v[0:3]\n"
"v_mfma_f32_16x16x32_bf16 v[12:15], v[224:227], v[212:215], v[12:15]\n"
"v_mfma_f32_16x16x32_bf16 v[24:27], v[224:227], v[216:219], v[24:27]\n"
"v_mfma_f32_16x16x32_bf16 v[36:39], v[224:227], v[220:223], v[36:39]\n"
"v_cmp_eq_u64 vcc, s[54:55], v[248:249]\n"
"s_mov_b32 s94, vcc_lo\n"
"s_bitcmp1_b32 s94, 3\n"
"s_cbranch_scc1 L_hd3\n"
"L_hp3:\n"
"buffer_load_dwordx2 v[248:249], v253, s[72:75], 0 offen sc1\n"
"s_waitcnt vmcnt(0)\n"
"v_cmp_eq_u64 vcc, s[54:55], v[248:249]\n"
"s_mov_b32 s94, vcc_lo\n"
"s_bitcmp1_b32 s94, 3\n"
"s_cbranch_scc1 L_hd3\n"
"s_sleep 2\n"
"s_sub_u32 s93, s93, 1\n"
"s_cbranch_scc0 L_hp3\n"
"s_mov_b32 s93, 0\n"
"L_hd3:\n"
"buffer_load_dwordx4 v[224:227], v254, s[72:75], s63 offen sc1\n"
"s_waitcnt vmcnt(5)\n"
"v_mfma_f32_16x16x32_bf16 v[4:7], v[228:231], v[208:211], v[4:7]\n"
"v_mfma_f32_16x16x32_bf16 v[16:19], v[228:231], v[212:215], v[16:19]\n"
"v_mfma_f32_16x16x32_bf16 v[28:31], v[228:231], v[216:219], v[28:31]\n"
"v_mfma_f32_16x16x32_bf16 v[40:43], v[228:231], v[220:223], v[40:43]\n"
"buffer_load_dwordx4 v[228:231], v254, s[72:75], s63 offen offset:1024 sc1\n"
"s_waitcnt vmcnt(5)\n"
"v_mfma_f32_16x16x32_bf16 v[8:11], v[232:235], v[208:211], v[8:11]\n"
"v_mfma_f32_16x16x32_bf16 v[20:23], v[232:235], v[212:215], v[20:23]\n"
"v_mfma_f32_16x16x32_bf16 v[32:35], v[232:235], v[216:219], v[32:35]\n"
"v_mfma_f32_16x16x32_bf16 v[44:47], v[232:235], v[220:223], v[44:47]\n"
"buffer_load_dwordx4 v[232:235], v254, s[72:75], s63 offen offset:2048 sc1\n"
"s_waitcnt vmcnt(5)\n"
"v_mfma_f32_16x16x32_bf16 v[0:3], v[236:239], v[48:51], v[0:3]\n"
"v_mfma_f32_16x16x32_bf16 v[12:15], v[236:239], v[52:55], v[12:15]\n"
"v_mfma_f32_16x16x32_bf16 v[24:27], v[236:239], v[56:59], v[24:27]\n"
"v_mfma_f32_16x16x32_bf16 v[36:39], v[236:239], v[60:63], v[36:39]\n"
"s_add_u32 s63, s63, 3072\n"
"buffer_load_dwordx4 v[236:239], v254, s[72:75], s63 offen sc1\n"
"s_waitcnt vmcnt(5)\n"
"v_mfma_f32_16x16x32_bf16 v[4:7], v[240:243], v[48:51], v[4:7]\n"
"v_mfma_f32_16x16x32_bf16 v[16:19], v[240:243], v[52:55], v[16:19]\n"
"v_mfma_f32_16x16x32_bf16 v[28:31], v[240:243], v[56:59], v[28:31]\n"
"v_mfma_f32_16x16x32_bf16 v[40:43], v[240:243], v[60:63], v[40:43]\n"
"buffer_load_dwordx4 v[240:243], v254, s[72:75], s63 offen offset:1024 sc1\n"
"s_waitcnt vmcnt(5)\n"
"v_mfma_f32_16x16x32_bf16 v[8:11], v[244:247], v[48:51], v[8:11]\n"
"v_mfma_f32_16x16x32_bf16 v[20:23], v[244:247], v[52:55], v[20:23]\n"
"v_mfma_f32_16x16x32_bf16 v[32:35], v[244:247], v[56:59], v[32:35]\n"
"v_mfma_f32_16x16x32_bf16 v[44:47], v[244:247], v[60:63], v[44:47]\n"
"buffer_load_dwordx4 v[244:247], v254, s[72:75], s63 offen offset:2048 sc1\n"
"s_waitcnt lgkmcnt(14)\n"
"s_waitcnt vmcnt(5)\n"
"v_mfma_f32_16x16x32_bf16 v[0:3], v[224:227], v[64:67], v[0:3]\n"
"v_mfma_f32_16x16x32_bf16 v[12:15], v[224:227], v[68:71], v[12:15]\n"
"v_mfma_f32_16x16x32_bf16 v[24:27], v[224:227], v[72:75], v[24:27]\n"
"v_mfma_f32_16x16x32_bf16 v[36:39], v[224:227], v[76:79], v[36:39]\n"
"s_add_u32 s63, s63, 3072\n"
"buffer_load_dwordx4 v[224:227], v254, s[72:75], s63 offen sc1\n"
"s_waitcnt vmcnt(5)\n"
"v_mfma_f32_16x16x32_bf16 v[4:7], v[228:231], v[64:67], v[4:7]\n"
"v_mfma_f32_16x16x32_bf16 v[16:19], v[228:231], v[68:71], v[16:19]\n"
"v_mfma_f32_16x16x32_bf16 v[28:31], v[228:231], v[72:75], v[28:31]\n"
"v_mfma_f32_16x16x32_bf16 v[40:43], v[228:231], v[76:79], v[40:43]\n"
"buffer_load_dwordx4 v[228:231], v254, s[72:75], s63 offen offset:1024 sc1\n"
"s_waitcnt vmcnt(5)\n"
"v_mfma_f32_16x16x32_bf16 v[8:11], v[232:235], v[64:67], v[8:11]\n"
"v_mfma_f32_16x16x32_bf16 v[20:23], v[232:235], v[68:71], v[20:23]\n"
"v_mfma_f32_16x16x32_bf16 v[32:35], v[232:235], v[72:75], v[32:35]\n"
"v_mfma_f32_16x16x32_bf16 v[44:47], v[232:235], v[76:79], v[44:47]\n"
"buffer_load_dwordx4 v[232:235], v254, s[72:75], s63 offen offset:2048 sc1\n"
"s_lshr_b32 s80, s51, 1\n"
"s_and_b32 s81, s51, 1\n"
"s_lshl_b32 s81, s81, 11\n"
"s_lshl_b32 s82, s80, 14\n"
"s_add_u32 s82, s82, s81\n"
"v_add_u32 v196, s82, v254\n"
"v_add_u32 v197, 0x10000, v196\n"
"v_bfe_u32 v198, v254, 4, 2\n"
"v_lshrrev_b32 v194, 8, v254\n"
"v_lshl_or_b32 v198, v194, 2, v198\n"
"v_lshlrev_b32 v198, 3, v198\n"
"v_and_b32 v199, 0xc0, v254\n"
"v_lshl_or_b32 v198, v199, 6, v198\n"
"s_mov_b32 s96, s82\n"
"s_waitcnt vmcnt(5)\n"
"v_mfma_f32_16x16x32_bf16 v[0:3], v[236:239], v[80:83], v[0:3]\n"
"v_mfma_f32_16x16x32_bf16 v[12:15], v[236:239], v[84:87], v[12:15]\n"
"v_mfma_f32_16x16x32_bf16 v[24:27], v[236:239], v[88:91], v[24:27]\n"
"v_mfma_f32_16x16x32_bf16 v[36:39], v[236:239], v[92:95], v[36:39]\n"
"s_add_u32 s63, s63, 3072\n"
"buffer_load_dwordx4 v[236:239], v254, s[72:75], s63 offen sc1\n"
"s_waitcnt vmcnt(5)\n"
"v_mfma_f32_16x16x32_bf16 v[4:7], v[240:243], v[80:83], v[4:7]\n"
"v_mfma_f32_16x16x32_bf16 v[16:19], v[240:243], v[84:87], v[16:19]\n"
"v_mfma_f32_16x16x32_bf16 v[28:31], v[240:243], v[88:91], v[28:31]\n"
"v_mfma_f32_16x16x32_bf16 v[40:43], v[240:243], v[92:95], v[40:43]\n"
"buffer_load_dwordx4 v[240:243], v254, s[72:75], s63 offen offset:1024 sc1\n"
"s_waitcnt vmcnt(5)\n"
"v_mfma_f32_16x16x32_bf16 v[8:11], v[244:247], v[80:83], v[8:11]\n"
"v_mfma_f32_16x16x32_bf16 v[20:23], v[244:247], v[84:87], v[20:23]\n"
"v_mfma_f32_16x16x32_bf16 v[32:35], v[244:247], v[88:91], v[32:35]\n"
"v_mfma_f32_16x16x32_bf16 v[44:47], v[244:247], v[92:95], v[44:47]\n"
"buffer_load_dwordx4 v[244:247], v254, s[72:75], s63 offen offset:2048 sc1\n"
"s_waitcnt vmcnt(5)\n"
"v_mfma_f32_16x16x32_bf16 v[0:3], v[224:227], v[96:99], v[0:3]\n"
"s_waitcnt lgkmcnt(12)\n"
"v_mfma_f32_16x16x32_bf16 v[12:15], v[224:227], v[100:103], v[12:15]\n"
"s_waitcnt lgkmcnt(10)\n"
"v_mfma_f32_16x16x32_bf16 v[24:27], v[224:227], v[104:107], v[24:27]\n"
"s_waitcnt lgkmcnt(8)\n"
"v_mfma_f32_16x16x32_bf16 v[36:39], v[224:227], v[108:111], v[36:39]\n"
"s_waitcnt vmcnt(4)\n"
"v_mfma_f32_16x16x32_bf16 v[4:7], v[228:231], v[96:99], v[4:7]\n"
"v_mfma_f32_16x16x32_bf16 v[16:19], v[228:231], v[100:103], v[16:19]\n"
"v_mfma_f32_16x16x32_bf16 v[28:31], v[228:231], v[104:107], v[28:31]\n"
"v_mfma_f32_16x16x32_bf16 v[40:43], v[228:231], v[108:111], v[40:43]\n"
"s_waitcnt vmcnt(3)\n"
"v_mfma_f32_16x16x32_bf16 v[8:11], v[232:235], v[96:99], v[8:11]\n"
"v_mfma_f32_16x16x32_bf16 v[20:23], v[232:235], v[100:103], v[20:23]\n"
"v_mfma_f32_16x16x32_bf16 v[32:35], v[232:235], v[104:107], v[32:35]\n"
"v_mfma_f32_16x16x32_bf16 v[44:47], v[232:235], v[108:111], v[44:47]\n"
"s_waitcnt lgkmcnt(6)\n"
"s_waitcnt vmcnt(2)\n"
"v_mfma_f32_16x16x32_bf16 v[0:3], v[236:239], v[112:115], v[0:3]\n"
"s_waitcnt lgkmcnt(4)\n"
"v_mfma_f32_16x16x32_bf16 v[12:15], v[236:239], v[116:119], v[12:15]\n"
"s_waitcnt lgkmcnt(2)\n"
"v_mfma_f32_16x16x32_bf16 v[24:27], v[236:239], v[120:123], v[24:27]\n"
"s_waitcnt lgkmcnt(0)\n"
"v_mfma_f32_16x16x32_bf16 v[36:39], v[236:239], v[124:127], v[36:39]\n"
"s_waitcnt vmcnt(1)\n"
"v_mfma_f32_16x16x32_bf16 v[4:7], v[240:243], v[112:115], v[4:7]\n"
"v_mfma_f32_16x16x32_bf16 v[16:19], v[240:243], v[116:119], v[16:19]\n"
"v_mfma_f32_16x16x32_bf16 v[28:31], v[240:243], v[120:123], v[28:31]\n"
"v_mfma_f32_16x16x32_bf16 v[40:43], v[240:243], v[124:127], v[40:43]\n"
"s_waitcnt vmcnt(0)\n"
"v_mfma_f32_16x16x32_bf16 v[8:11], v[244:247], v[112:115], v[8:11]\n"
"v_mfma_f32_16x16x32_bf16 v[20:23], v[244:247], v[116:119], v[20:23]\n"
"v_mfma_f32_16x16x32_bf16 v[32:35], v[244:247], v[120:123], v[32:35]\n"
"v_mfma_f32_16x16x32_bf16 v[44:47], v[244:247], v[124:127], v[44:47]\n"
"s_add_u32 m0, s95, 32768\n"
"s_nop 0\n"
"ds_write_addtid_b32 v0\n"
"ds_write_addtid_b32 v1 offset:1024\n"
"ds_write_addtid_b32 v2 offset:2048\n"
"ds_write_addtid_b32 v3 offset:3072\n"
"ds_write_addtid_b32 v12 offset:256\n"
"ds_write_addtid_b32 v13 offset:1280\n"
"ds_write_addtid_b32 v14 offset:2304\n"
"ds_write_addtid_b32 v15 offset:3328\n"
"ds_write_addtid_b32 v24 offset:512\n"
"ds_write_addtid_b32 v25 offset:1536\n"
"ds_write_addtid_b32 v26 offset:2560\n"
"ds_write_addtid_b32 v27 offset:3584\n"
"ds_write_addtid_b32 v36 offset:768\n"
"ds_write_addtid_b32 v37 offset:1792\n"
"ds_write_addtid_b32 v38 offset:2816\n"
"ds_write_addtid_b32 v39 offset:3840\n"
"ds_write_addtid_b32 v4 offset:16384\n"
"ds_write_addtid_b32 v5 offset:17408\n"
"ds_write_addtid_b32 v6 offset:18432\n"
"ds_write_addtid_b32 v7 offset:19456\n"
"ds_write_addtid_b32 v16 offset:16640\n"
"ds_write_addtid_b32 v17 offset:17664\n"
"ds_write_addtid_b32 v18 offset:18688\n"
"ds_write_addtid_b32 v19 offset:19712\n"
"ds_write_addtid_b32 v28 offset:16896\n"
"ds_write_addtid_b32 v29 offset:17920\n"
"ds_write_addtid_b32 v30 offset:18944\n"
"ds_write_addtid_b32 v31 offset:19968\n"
"ds_write_addtid_b32 v40 offset:17152\n"
"ds_write_addtid_b32 v41 offset:18176\n"
"ds_write_addtid_b32 v42 offset:19200\n"
"ds_write_addtid_b32 v43 offset:20224\n"
"ds_write_addtid_b32 v8 offset:32768\n"
"ds_write_addtid_b32 v9 offset:33792\n"
"ds_write_addtid_b32 v10 offset:34816\n"
"ds_write_addtid_b32 v11 offset:35840\n"
"ds_write_addtid_b32 v20 offset:33024\n"
"ds_write_addtid_b32 v21 offset:34048\n"
"ds_write_addtid_b32 v22 offset:35072\n"
"ds_write_addtid_b32 v23 offset:36096\n"
"ds_write_addtid_b32 v32 offset:33280\n"
"ds_write_addtid_b32 v33 offset:34304\n"
"ds_write_addtid_b32 v34 offset:35328\n"
"ds_write_addtid_b32 v35 offset:36352\n"
"ds_write_addtid_b32 v44 offset:33536\n"
"ds_write_addtid_b32 v45 offset:34560\n"
"ds_write_addtid_b32 v46 offset:35584\n"
"ds_write_addtid_b32 v47 offset:36608\n"
"s_waitcnt lgkmcnt(0)\n"
"s_branch L_red\n"
"L_hpace:\n"
"s_memrealtime s[82:83]\n"
"s_waitcnt lgkmcnt(0)\n"
"s_lshl2_add_u32 s80, s82, s94\n"
"s_cmp_ge_i32 s80, 0\n"
"s_cbranch_scc1 L_pa1\n"
"L_pa0:\n"
"s_sleep 1\n"
"s_add_u32 s80, s80, 18\n"
"s_cbranch_scc0 L_pa0\n"
"L_pa1:\n"
"s_sub_u32 s80, s80, -204\n"
"s_max_i32 s80, s80, 228\n"
"s_sub_u32 s94, s94, s80\n"
"s_sub_u32 s92, s92, 244\n"
"s_sub_u32 s80, s92, s94\n"
"s_min_i32 s80, s80, 0\n"
"s_add_u32 s94, s94, s80\n"
"s_setpc_b64 s[98:99]\n"
"L_spinFR:\n"
"s_sleep 1\n"
"L_spinFR0:\n"
"ds_read_b32 v13, v11\n"
"s_sub_u32 s93, s93, 1\n"
"s_cbranch_scc1 L_spinFRto\n"
"s_waitcnt lgkmcnt(0)\n"
"v_cmp_gt_u32 vcc, s80, v13\n"
"s_cbranch_vccnz L_spinFR\n"
"L_spinFRto:\n"
"s_max_i32 s93, s93, 0\n"
"s_waitcnt lgkmcnt(0)\n"
"s_setpc_b64 s[98:99]\n"
"L_compute:\n"
"s_lshl_b32 m0, s53, 9\n"
"v_bfe_u32 v240, v255, 2, 2\n"
"v_lshlrev_b32 v240, 1, v240\n"
"v_mov_b32 v241, 0x78\n"
"v_lshrrev_b32 v240, v240, v241\n"
"v_lshrrev_b32 v241, 4, v255\n"
"v_xor_b32 v240, v240, v241\n"
"v_and_b32 v240, 3, v240\n"
"v_lshlrev_b32 v240, 4, v240\n"
"v_and_b32 v241, 7, v255\n"
"v_lshl_or_b32 v240, v241, 6, v240\n"
"v_bfe_u32 v241, v255, 3, 1\n"
"v_lshl_or_b32 v248, v241, 11, v240\n"
"v_add_u32 v248, 98304, v248\n"
"v_lshl_or_b32 v176, v241, 10, v240\n"
"s_mul_i32 s80, s53, 6144\n"
"s_add_u32 s80, s80, 65536\n"
"v_add_u32 v176, s80, v176\n"
"v_bfe_u32 v240, v255, 4, 1\n"
"v_mul_u32_u24 v240, 3, v240\n"
"v_and_b32 v241, 3, v255\n"
"v_xor_b32 v240, v240, v241\n"
"v_lshlrev_b32 v240, 4, v240\n"
"v_and_b32 v241, 1, v255\n"
"v_lshl_or_b32 v240, v241, 3, v240\n"
"v_bfe_u32 v241, v255, 2, 2\n"
"v_lshl_or_b32 v240, v241, 6, v240\n"
"v_lshrrev_b32 v241, 4, v255\n"
"v_lshl_or_b32 v240, v241, 11, v240\n"
"v_add_u32 v172, 49152, v240\n"
"v_xor_b32 v173, 0x120, v172\n"
"v_xor_b32 v174, 8, v172\n"
"v_xor_b32 v175, 0x128, v172\n"
"v_lshrrev_b32 v240, 4, v255\n"
"v_lshlrev_b32 v241, 1, v240\n"
"v_mov_b32 v242, 0x78\n"
"v_lshrrev_b32 v241, v241, v242\n"
"v_bfe_u32 v242, v255, 1, 1\n"
"v_xor_b32 v241, v241, v242\n"
"v_and_b32 v241, 3, v241\n"
"v_lshlrev_b32 v241, 4, v241\n"
"v_and_b32 v242, 1, v255\n"
"v_lshl_or_b32 v241, v242, 3, v241\n"
"v_bfe_u32 v242, v255, 2, 3\n"
"v_lshl_or_b32 v241, v242, 6, v241\n"
"v_lshrrev_b32 v242, 5, v255\n"
"v_lshl_or_b32 v249, v242, 11, v241\n"
"v_add_u32 v249, 106496, v249\n"
"v_xor_b32 v250, 32, v249\n"
"s_lshl2_add_u32 s80, s84, s53\n"
"s_lshl_b32 s80, s80, 5\n"
"s_add_u32 s80, s80, s85\n"
"s_mul_i32 s81, s80, 12288\n"
"s_add_u32 s64, s81, 262144\n"
"s_lshl_b32 s80, s80, 7\n"
"s_add_u32 s65, s80, 12845056\n"
"s_waitcnt lgkmcnt(0)\n"
"s_mov_b64 s[72:73], s[48:49]\n"
"s_lshl_b32 s80, s87, 7\n"
"s_mul_i32 s81, s86, 49152\n"
"s_add_u32 s80, s80, s81\n"
"s_add_u32 s76, s46, s80\n"
"s_addc_u32 s77, s47, 0\n"
"s_sub_u32 s81, 1572864, s80\n"
"s_mov_b32 s78, s81\n"
"s_mov_b32 s79, 0x20000\n"
"s_bfe_u32 s80, s88, 0x10002\n"
"s_lshl1_add_u32 s80, s80, s51\n"
"s_and_b32 s80, s80, 3\n"
"s_and_b32 s81, s88, 3\n"
"s_lshl2_add_u32 s80, s81, s80\n"
"s_xor_b32 s82, s80, 16\n"
"s_lshl_b32 s83, s82, 10\n"
"s_add_u32 s83, s83, s87\n"
"s_lshl_b32 s56, s83, 7\n"
"s_lshl_b32 s83, s86, 5\n"
"s_add_u32 s83, s83, s82\n"
"s_lshl_b32 s81, s83, 7\n"
"s_add_u32 s81, s81, 12845056\n"
"v_mov_b32 v253, s81\n"
"s_mul_i32 s83, s83, 12288\n"
"s_add_u32 s60, s83, 262144\n"
"s_xor_b32 s82, s80, 20\n"
"s_lshl_b32 s83, s82, 10\n"
"s_add_u32 s83, s83, s87\n"
"s_lshl_b32 s57, s83, 7\n"
"s_lshl_b32 s83, s86, 5\n"
"s_add_u32 s83, s83, s82\n"
"s_lshl_b32 s81, s83, 7\n"
"s_add_u32 s81, s81, 12845056\n"
"v_writelane_b32 v253, s81, 1\n"
"s_mul_i32 s83, s83, 12288\n"
"s_add_u32 s61, s83, 262144\n"
"s_xor_b32 s82, s80, 24\n"
"s_lshl_b32 s83, s82, 10\n"
"s_add_u32 s83, s83, s87\n"
"s_lshl_b32 s58, s83, 7\n"
"s_lshl_b32 s83, s86, 5\n"
"s_add_u32 s83, s83, s82\n"
"s_lshl_b32 s81, s83, 7\n"
"s_add_u32 s81, s81, 12845056\n"
"v_writelane_b32 v253, s81, 2\n"
"s_mul_i32 s83, s83, 12288\n"
"s_add_u32 s62, s83, 262144\n"
"s_xor_b32 s82, s80, 28\n"
"s_lshl_b32 s83, s82, 10\n"
"s_add_u32 s83, s83, s87\n"
"s_lshl_b32 s59, s83, 7\n"
"s_lshl_b32 s83, s86, 5\n"
"s_add_u32 s83, s83, s82\n"
"s_lshl_b32 s81, s83, 7\n"
"s_add_u32 s81, s81, 12845056\n"
"v_writelane_b32 v253, s81, 3\n"
"s_mul_i32 s83, s83, 12288\n"
"s_add_u32 s63, s83, 262144\n"
"v_lshrrev_b32 v243, 4, v255\n"
"v_lshlrev_b32 v244, 1, v243\n"
"v_mov_b32 v245, 0x78\n"
"v_lshrrev_b32 v244, v244, v245\n"
"v_bfe_u32 v245, v255, 1, 1\n"
"v_xor_b32 v244, v244, v245\n"
"v_and_b32 v244, 3, v244\n"
"v_lshlrev_b32 v244, 4, v244\n"
"v_and_b32 v245, 1, v255\n"
"v_lshl_or_b32 v244, v245, 3, v244\n"
"v_bfe_u32 v245, v255, 2, 3\n"
"v_lshl_or_b32 v244, v245, 6, v244\n"
"v_lshrrev_b32 v245, 5, v255\n"
"v_lshl_or_b32 v255, v245, 10, v244\n"
"s_add_u32 s80, s95, 81920\n"
"v_add_u32 v255, s80, v255\n"
"s_cmp_eq_u32 s52, 0\n"
"s_cbranch_scc0 L_pretail\n"
"v_mov_b32 v251, 0\n"
"ds_write_addtid_b32 v251\n"
"v_mov_b32 v0, 0\n"
"v_mov_b32 v1, 0\n"
"v_mov_b32 v2, 0\n"
"v_mov_b32 v3, 0\n"
"v_mov_b32 v4, 0\n"
"v_mov_b32 v5, 0\n"
"v_mov_b32 v6, 0\n"
"v_mov_b32 v7, 0\n"
"v_mov_b32 v8, 0\n"
"v_mov_b32 v9, 0\n"
"v_mov_b32 v10, 0\n"
"v_mov_b32 v11, 0\n"
"v_mov_b32 v12, 0\n"
"v_mov_b32 v13, 0\n"
"v_mov_b32 v14, 0\n"
"v_mov_b32 v15, 0\n"
"v_mov_b32 v16, 0\n"
"v_mov_b32 v17, 0\n"
"v_mov_b32 v18, 0\n"
"v_mov_b32 v19, 0\n"
"v_mov_b32 v20, 0\n"
"v_mov_b32 v21, 0\n"
"v_mov_b32 v22, 0\n"
"v_mov_b32 v23, 0\n"
"v_mov_b32 v24, 0\n"
"v_mov_b32 v25, 0\n"
"v_mov_b32 v26, 0\n"
"v_mov_b32 v27, 0\n"
"v_mov_b32 v28, 0\n"
"v_mov_b32 v29, 0\n"
"v_mov_b32 v30, 0\n"
"v_mov_b32 v31, 0\n"
"v_mov_b32 v32, 0\n"
"v_mov_b32 v33, 0\n"
"v_mov_b32 v34, 0\n"
"v_mov_b32 v35, 0\n"
"v_mov_b32 v36, 0\n"
"v_mov_b32 v37, 0\n"
"v_mov_b32 v38, 0\n"
"v_mov_b32 v39, 0\n"
"v_mov_b32 v40, 0\n"
"v_mov_b32 v41, 0\n"
"v_mov_b32 v42, 0\n"
"v_mov_b32 v43, 0\n"
"v_mov_b32 v44, 0\n"
"v_mov_b32 v45, 0\n"
"v_mov_b32 v46, 0\n"
"v_mov_b32 v47, 0\n"
"v_mov_b32 v48, 0\n"
"v_mov_b32 v49, 0\n"
"v_mov_b32 v50, 0\n"
"v_mov_b32 v51, 0\n"
"v_mov_b32 v52, 0\n"
"v_mov_b32 v53, 0\n"
"v_mov_b32 v54, 0\n"
"v_mov_b32 v55, 0\n"
"v_mov_b32 v56, 0\n"
"v_mov_b32 v57, 0\n"
"v_mov_b32 v58, 0\n"
"v_mov_b32 v59, 0\n"
"v_mov_b32 v60, 0\n"
"v_mov_b32 v61, 0\n"
"v_mov_b32 v62, 0\n"
"v_mov_b32 v63, 0\n"
"v_mov_b32 v64, 0\n"
"v_mov_b32 v65, 0\n"
"v_mov_b32 v66, 0\n"
"v_mov_b32 v67, 0\n"
"v_mov_b32 v68, 0\n"
"v_mov_b32 v69, 0\n"
"v_mov_b32 v70, 0\n"
"v_mov_b32 v71, 0\n"
"v_mov_b32 v72, 0\n"
"v_mov_b32 v73, 0\n"
"v_mov_b32 v74, 0\n"
"v_mov_b32 v75, 0\n"
"v_mov_b32 v76, 0\n"
"v_mov_b32 v77, 0\n"
"v_mov_b32 v78, 0\n"
"v_mov_b32 v79, 0\n"
"v_mov_b32 v80, 0\n"
"v_mov_b32 v81, 0\n"
"v_mov_b32 v82, 0\n"
"v_mov_b32 v83, 0\n"
"v_mov_b32 v84, 0\n"
"v_mov_b32 v85, 0\n"
"v_mov_b32 v86, 0\n"
"v_mov_b32 v87, 0\n"
"v_mov_b32 v88, 0\n"
"v_mov_b32 v89, 0\n"
"v_mov_b32 v90, 0\n"
"v_mov_b32 v91, 0\n"
"v_mov_b32 v92, 0\n"
"v_mov_b32 v93, 0\n"
"v_mov_b32 v94, 0\n"
"v_mov_b32 v95, 0\n"
"v_mov_b32 v168, 0x3f803f80\n"
"v_mov_b32 v169, 0x3f803f80\n"
"v_mov_b32 v170, 0x3f803f80\n"
"v_mov_b32 v171, 0x3f803f80\n"
"v_lshrrev_b32 v169, 4, v254\n"
"v_xor_b32 v169, v169, v254\n"
"v_and_b32 v169, 32, v169\n"
"v_cmp_eq_u32 vcc, 0, v169\n"
"s_nop 1\n"
"v_cndmask_b32 v171, 0, v168, vcc\n"
"v_cndmask_b32 v170, 0, v168, vcc\n"
"v_cndmask_b32 v169, 0, v168, vcc\n"
"v_cndmask_b32 v168, 0, v168, vcc\n"
"v_bfe_i32 v96, v254, 4, 1\n"
"v_and_b32 v96, 0xaaaa0000, v96\n"
"v_or_b32 v96, 0x44440000, v96\n"
"v_or_b32 v254, v96, v254\n"
"v_mov_b32 v96, 0\n"
"v_mov_b32 v97, 0\n"
"v_mov_b32 v98, 0\n"
"v_mov_b32 v99, 0\n"
"v_mov_b32 v100, 0\n"
"v_mov_b32 v101, 0\n"
"v_mov_b32 v102, 0\n"
"v_mov_b32 v103, 0\n"
"v_mov_b32 v104, 0\n"
"v_mov_b32 v105, 0\n"
"v_mov_b32 v106, 0\n"
"v_mov_b32 v107, 0\n"
"s_mov_b32 s66, 0\n"
"s_mov_b32 s96, 0\n"
"s_waitcnt vmcnt(0)\n"
"v_mov_b32 v236, 0\n"
"v_mov_b32 v237, 0\n"
"v_mov_b32 v238, 0\n"
"v_mov_b32 v239, 0\n"
"v_mov_b32 v240, 0\n"
"v_mov_b32 v241, 0\n"
"v_mov_b32 v242, 0\n"
"v_mov_b32 v243, 0\n"
"v_mov_b32 v244, 0\n"
"v_mov_b32 v245, 0\n"
"v_mov_b32 v246, 0\n"
"v_mov_b32 v247, 0\n"
"s_waitcnt lgkmcnt(0)\n"
"s_barrier\n"
"ds_read_b64_tr_b16 v[204:205], v172 offset:0\n"
"ds_read_b64_tr_b16 v[206:207], v173 offset:0\n"
"ds_read_b128 v[180:183], v176 offset:0\n"
"ds_read_b64_tr_b16 v[208:209], v174 offset:0\n"
"ds_read_b64_tr_b16 v[210:211], v175 offset:0\n"
"ds_read_b128 v[184:187], v176 offset:2048\n"
"ds_read_b64_tr_b16 v[212:213], v172 offset:512\n"
"ds_read_b64_tr_b16 v[214:215], v173 offset:512\n"
"ds_read_b128 v[188:191], v176 offset:4096\n"
"ds_read_b64_tr_b16 v[216:217], v174 offset:512\n"
"ds_read_b64_tr_b16 v[218:219], v175 offset:512\n"
"ds_read_b64_tr_b16 v[220:221], v172 offset:1024\n"
"ds_read_b64_tr_b16 v[222:223], v173 offset:1024\n"
"ds_read_b64_tr_b16 v[224:225], v174 offset:1024\n"
"ds_read_b64_tr_b16 v[226:227], v175 offset:1024\n"
"ds_read_b64_tr_b16 v[228:229], v172 offset:1536\n"
"ds_read_b64_tr_b16 v[230:231], v173 offset:1536\n"
"ds_read_b64_tr_b16 v[232:233], v174 offset:1536\n"
"s_branch L_qmid\n"
"L_qloop:\n"
"v_mfma_f32_16x16x32_bf16 v[12:15], v[208:211], v[192:195], v[12:15]\n"
"v_mfma_f32_16x16x32_bf16 v[16:19], v[208:211], v[196:199], v[16:19]\n"
"s_waitcnt lgkmcnt(0)\n"
"s_barrier\n"
"ds_read_b64_tr_b16 v[204:205], v172 offset:0\n"
"ds_read_b64_tr_b16 v[206:207], v173 offset:0\n"
"v_mfma_f32_16x16x32_bf16 v[20:23], v[208:211], v[200:203], v[20:23]\n"
"ds_read_b128 v[180:183], v176 offset:0\n"
"ds_read_b64_tr_b16 v[208:209], v174 offset:0\n"
"v_mfma_f32_16x16x32_bf16 v[24:27], v[212:215], v[192:195], v[24:27]\n"
"ds_read_b64_tr_b16 v[210:211], v175 offset:0\n"
"v_mfma_f32_16x16x32_bf16 v[28:31], v[212:215], v[196:199], v[28:31]\n"
"ds_read_b128 v[184:187], v176 offset:2048\n"
"v_mfma_f32_16x16x32_bf16 v[32:35], v[212:215], v[200:203], v[32:35]\n"
"ds_read_b64_tr_b16 v[212:213], v172 offset:512\n"
"v_mfma_f32_16x16x32_bf16 v[36:39], v[216:219], v[192:195], v[36:39]\n"
"ds_read_b64_tr_b16 v[214:215], v173 offset:512\n"
"v_mfma_f32_16x16x32_bf16 v[40:43], v[216:219], v[196:199], v[40:43]\n"
"ds_read_b128 v[188:191], v176 offset:4096\n"
"v_mfma_f32_16x16x32_bf16 v[44:47], v[216:219], v[200:203], v[44:47]\n"
"ds_read_b64_tr_b16 v[216:217], v174 offset:512\n"
"v_mfma_f32_16x16x32_bf16 v[48:51], v[220:223], v[192:195], v[48:51]\n"
"ds_read_b64_tr_b16 v[218:219], v175 offset:512\n"
"v_mfma_f32_16x16x32_bf16 v[52:55], v[220:223], v[196:199], v[52:55]\n"
"v_mfma_f32_16x16x32_bf16 v[56:59], v[220:223], v[200:203], v[56:59]\n"
"ds_read_b64_tr_b16 v[220:221], v172 offset:1024\n"
"v_mfma_f32_16x16x32_bf16 v[60:63], v[224:227], v[192:195], v[60:63]\n"
"ds_read_b64_tr_b16 v[222:223], v173 offset:1024\n"
"v_mfma_f32_16x16x32_bf16 v[64:67], v[224:227], v[196:199], v[64:67]\n"
"v_mfma_f32_16x16x32_bf16 v[68:71], v[224:227], v[200:203], v[68:71]\n"
"ds_read_b64_tr_b16 v[224:225], v174 offset:1024\n"
"v_mfma_f32_16x16x32_bf16 v[72:75], v[228:231], v[192:195], v[72:75]\n"
"ds_read_b64_tr_b16 v[226:227], v175 offset:1024\n"
"v_mfma_f32_16x16x32_bf16 v[76:79], v[228:231], v[196:199], v[76:79]\n"
"v_mfma_f32_16x16x32_bf16 v[80:83], v[228:231], v[200:203], v[80:83]\n"
"ds_read_b64_tr_b16 v[228:229], v172 offset:1536\n"
"v_mfma_f32_16x16x32_bf16 v[84:87], v[232:235], v[192:195], v[84:87]\n"
"ds_read_b64_tr_b16 v[230:231], v173 offset:1536\n"
"v_mfma_f32_16x16x32_bf16 v[88:91], v[232:235], v[196:199], v[88:91]\n"
"v_mfma_f32_16x16x32_bf16 v[92:95], v[232:235], v[200:203], v[92:95]\n"
"ds_read_b64_tr_b16 v[232:233], v174 offset:1536\n"
"L_qmid:\n"
"s_waitcnt lgkmcnt(14)\n"
"v_mfma_f32_16x16x32_bf16 v[0:3], v[204:207], v[180:183], v[0:3]\n"
"ds_read_b64_tr_b16 v[234:235], v175 offset:1536\n"
"s_waitcnt lgkmcnt(13)\n"
"v_mfma_f32_16x16x32_bf16 v[4:7], v[204:207], v[184:187], v[4:7]\n"
"ds_read_b128 v[192:195], v176 offset:512\n"
"s_waitcnt lgkmcnt(11)\n"
"v_mfma_f32_16x16x32_bf16 v[8:11], v[204:207], v[188:191], v[8:11]\n"
"ds_read_b64_tr_b16 v[204:205], v172 offset:8192\n"
"s_cmp_eq_u32 s96, 1\n"
"s_cselect_b32 s80, 2, -1\n"
"v_mfma_f32_16x16x32_bf16 v[12:15], v[208:211], v[180:183], v[12:15]\n"
"ds_read_b64_tr_b16 v[206:207], v173 offset:8192\n"
"s_cselect_b32 s96, -2, s96\n"
"s_add_u32 s96, s96, 1\n"
"v_mfma_f32_16x16x32_bf16 v[16:19], v[208:211], v[184:187], v[16:19]\n"
"ds_read_b128 v[196:199], v176 offset:2560\n"
"v_mfma_f32_16x16x32_bf16 v[20:23], v[208:211], v[188:191], v[20:23]\n"
"ds_read_b64_tr_b16 v[208:209], v174 offset:8192\n"
"s_mul_i32 s80, s80, 40960\n"
"v_mfma_f32_16x16x32_bf16 v[24:27], v[212:215], v[180:183], v[24:27]\n"
"ds_read_b64_tr_b16 v[210:211], v175 offset:8192\n"
"s_add_u32 s66, s66, 1\n"
"s_cmp_lt_u32 s66, 8\n"
"v_mfma_f32_16x16x32_bf16 v[28:31], v[212:215], v[184:187], v[28:31]\n"
"ds_read_b128 v[200:203], v176 offset:4608\n"
"v_mfma_f32_16x16x32_bf16 v[32:35], v[212:215], v[188:191], v[32:35]\n"
"ds_read_b64_tr_b16 v[212:213], v172 offset:8704\n"
"s_waitcnt lgkmcnt(14)\n"
"v_mfma_f32_16x16x32_bf16 v[36:39], v[216:219], v[180:183], v[36:39]\n"
"ds_read_b64_tr_b16 v[214:215], v173 offset:8704\n"
"v_mfma_f32_16x16x32_bf16 v[40:43], v[216:219], v[184:187], v[40:43]\n"
"v_add_u32 v251, 1, v251\n"
"v_add_u32 v176, s80, v176\n"
"v_mfma_f32_16x16x32_bf16 v[44:47], v[216:219], v[188:191], v[44:47]\n"
"ds_read_b64_tr_b16 v[216:217], v174 offset:8704\n"
"v_mfma_f32_16x16x32_bf16 v[48:51], v[220:223], v[180:183], v[48:51]\n"
"ds_read_b64_tr_b16 v[218:219], v175 offset:8704\n"
"v_mfma_f32_16x16x32_bf16 v[52:55], v[220:223], v[184:187], v[52:55]\n"
"v_mfma_f32_16x16x32_bf16 v[56:59], v[220:223], v[188:191], v[56:59]\n"
"ds_read_b64_tr_b16 v[220:221], v172 offset:9216\n"
"s_waitcnt lgkmcnt(14)\n"
"v_mfma_f32_16x16x32_bf16 v[60:63], v[224:227], v[180:183], v[60:63]\n"
"ds_read_b64_tr_b16 v[222:223], v173 offset:9216\n"
"v_mfma_f32_16x16x32_bf16 v[64:67], v[224:227], v[184:187], v[64:67]\n"
"v_mfma_f32_16x16x32_bf16 v[68:71], v[224:227], v[188:191], v[68:71]\n"
"ds_read_b64_tr_b16 v[224:225], v174 offset:9216\n"
"v_mfma_f32_16x16x32_bf16 v[72:75], v[228:231], v[180:183], v[72:75]\n"
"ds_read_b64_tr_b16 v[226:227], v175 offset:9216\n"
"v_mfma_f32_16x16x32_bf16 v[76:79], v[228:231], v[184:187], v[76:79]\n"
"v_mfma_f32_16x16x32_bf16 v[80:83], v[228:231], v[188:191], v[80:83]\n"
"ds_read_b64_tr_b16 v[228:229], v172 offset:9728\n"
"s_waitcnt lgkmcnt(14)\n"
"v_mfma_f32_16x16x32_bf16 v[84:87], v[232:235], v[180:183], v[84:87]\n"
"ds_read_b64_tr_b16 v[230:231], v173 offset:9728\n"
"v_mfma_f32_16x16x32_bf16 v[88:91], v[232:235], v[184:187], v[88:91]\n"
"v_add_u32 v172, s80, v172\n"
"v_add_u32 v173, s80, v173\n"
"v_mfma_f32_16x16x32_bf16 v[92:95], v[232:235], v[188:191], v[92:95]\n"
"ds_read_b64_tr_b16 v[232:233], v174 offset:9728\n"
"s_waitcnt lgkmcnt(14)\n"
"v_mfma_f32_16x16x32_bf16 v[0:3], v[204:207], v[192:195], v[0:3]\n"
"ds_read_b64_tr_b16 v[234:235], v175 offset:9728\n"
"v_mfma_f32_16x16x32_bf16 v[4:7], v[204:207], v[196:199], v[4:7]\n"
"v_add_u32 v174, s80, v174\n"
"v_add_u32 v175, s80, v175\n"
"s_waitcnt lgkmcnt(12)\n"
"v_mfma_f32_16x16x32_bf16 v[8:11], v[204:207], v[200:203], v[8:11]\n"
"s_cbranch_scc1 L_qloop\n"
"s_mov_b32 s80, 0x3e0293ee\n"
"s_mov_b32 s81, s80\n"
"v_mfma_f32_16x16x32_bf16 v[12:15], v[208:211], v[192:195], v[12:15]\n"
"ds_write_addtid_b32 v251\n"
"v_mfma_f32_16x16x32_bf16 v[16:19], v[208:211], v[196:199], v[16:19]\n"
"v_mfma_f32_16x16x32_bf16 v[20:23], v[208:211], v[200:203], v[20:23]\n"
"v_mul_f32 v0, s80, v0\n"
"v_mul_f32 v1, s80, v1\n"
"v_mul_f32 v2, s80, v2\n"
"s_waitcnt lgkmcnt(11)\n"
"v_mfma_f32_16x16x32_bf16 v[24:27], v[212:215], v[192:195], v[24:27]\n"
"v_mul_f32 v3, s80, v3\n"
"v_cvt_pk_bf16_f32 v108, v0, v1\n"
"v_cvt_pk_bf16_f32 v109, v2, v3\n"
"v_mfma_f32_16x16x32_bf16 v[28:31], v[212:215], v[196:199], v[28:31]\n"
"v_mul_f32 v12, s80, v12\n"
"v_mul_f32 v13, s80, v13\n"
"v_mul_f32 v14, s80, v14\n"
"v_mfma_f32_16x16x32_bf16 v[32:35], v[212:215], v[200:203], v[32:35]\n"
"v_mul_f32 v15, s80, v15\n"
"v_cvt_pk_bf16_f32 v110, v12, v13\n"
"v_cvt_pk_bf16_f32 v111, v14, v15\n"
"s_waitcnt lgkmcnt(9)\n"
"v_mfma_f32_16x16x32_bf16 v[36:39], v[216:219], v[192:195], v[36:39]\n"
"v_mul_f32 v4, s80, v4\n"
"v_mul_f32 v5, s80, v5\n"
"v_mul_f32 v6, s80, v6\n"
"v_mfma_f32_16x16x32_bf16 v[40:43], v[216:219], v[196:199], v[40:43]\n"
"v_mul_f32 v7, s80, v7\n"
"v_cvt_pk_bf16_f32 v124, v4, v5\n"
"v_cvt_pk_bf16_f32 v125, v6, v7\n"
"v_mfma_f32_16x16x32_bf16 v[44:47], v[216:219], v[200:203], v[44:47]\n"
"v_mul_f32 v16, s80, v16\n"
"v_mul_f32 v17, s80, v17\n"
"v_mul_f32 v18, s80, v18\n"
"s_waitcnt lgkmcnt(7)\n"
"v_mfma_f32_16x16x32_bf16 v[48:51], v[220:223], v[192:195], v[48:51]\n"
"v_mul_f32 v19, s80, v19\n"
"v_cvt_pk_bf16_f32 v126, v16, v17\n"
"v_cvt_pk_bf16_f32 v127, v18, v19\n"
"v_mfma_f32_16x16x32_bf16 v[52:55], v[220:223], v[196:199], v[52:55]\n"
"v_mul_f32 v8, s80, v8\n"
"v_mul_f32 v9, s80, v9\n"
"v_mul_f32 v10, s80, v10\n"
"v_mfma_f32_16x16x32_bf16 v[56:59], v[220:223], v[200:203], v[56:59]\n"
"v_mul_f32 v11, s80, v11\n"
"v_cvt_pk_bf16_f32 v140, v8, v9\n"
"v_cvt_pk_bf16_f32 v141, v10, v11\n"
"s_waitcnt lgkmcnt(5)\n"
"v_mfma_f32_16x16x32_bf16 v[60:63], v[224:227], v[192:195], v[60:63]\n"
"v_mul_f32 v20, s80, v20\n"
"v_mul_f32 v21, s80, v21\n"
"v_mul_f32 v22, s80, v22\n"
"v_mfma_f32_16x16x32_bf16 v[64:67], v[224:227], v[196:199], v[64:67]\n"
"v_mul_f32 v23, s80, v23\n"
"v_cvt_pk_bf16_f32 v142, v20, v21\n"
"v_cvt_pk_bf16_f32 v143, v22, v23\n"
"v_mfma_f32_16x16x32_bf16 v[68:71], v[224:227], v[200:203], v[68:71]\n"
"v_mul_f32 v24, s80, v24\n"
"v_mul_f32 v25, s80, v25\n"
"v_mul_f32 v26, s80, v26\n"
"s_waitcnt lgkmcnt(3)\n"
"v_mfma_f32_16x16x32_bf16 v[72:75], v[228:231], v[192:195], v[72:75]\n"
"v_mul_f32 v27, s80, v27\n"
"v_cvt_pk_bf16_f32 v112, v24, v25\n"
"v_cvt_pk_bf16_f32 v113, v26, v27\n"
"v_mfma_f32_16x16x32_bf16 v[76:79], v[228:231], v[196:199], v[76:79]\n"
"v_mul_f32 v36, s80, v36\n"
"v_mul_f32 v37, s80, v37\n"
"v_mul_f32 v38, s80, v38\n"
"v_mfma_f32_16x16x32_bf16 v[80:83], v[228:231], v[200:203], v[80:83]\n"
"v_mul_f32 v39, s80, v39\n"
"v_cvt_pk_bf16_f32 v114, v36, v37\n"
"v_cvt_pk_bf16_f32 v115, v38, v39\n"
"s_waitcnt lgkmcnt(1)\n"
"v_mfma_f32_16x16x32_bf16 v[84:87], v[232:235], v[192:195], v[84:87]\n"
"v_mul_f32 v28, s80, v28\n"
"v_mul_f32 v29, s80, v29\n"
"v_mul_f32 v30, s80, v30\n"
"v_mfma_f32_16x16x32_bf16 v[88:91], v[232:235], v[196:199], v[88:91]\n"
"v_mul_f32 v31, s80, v31\n"
"v_cvt_pk_bf16_f32 v128, v28, v29\n"
"v_cvt_pk_bf16_f32 v129, v30, v31\n"
"v_mfma_f32_16x16x32_bf16 v[92:95], v[232:235], v[200:203], v[92:95]\n"
"v_mul_f32 v40, s80, v40\n"
"v_mul_f32 v41, s80, v41\n"
"v_mul_f32 v42, s80, v42\n"
"v_mul_f32 v43, s80, v43\n"
"v_cvt_pk_bf16_f32 v130, v40, v41\n"
"v_cvt_pk_bf16_f32 v131, v42, v43\n"
"v_mul_f32 v32, s80, v32\n"
"v_mul_f32 v33, s80, v33\n"
"v_mul_f32 v34, s80, v34\n"
"v_mul_f32 v35, s80, v35\n"
"v_cvt_pk_bf16_f32 v144, v32, v33\n"
"v_cvt_pk_bf16_f32 v145, v34, v35\n"
"v_mul_f32 v44, s80, v44\n"
"v_mul_f32 v45, s80, v45\n"
"v_mul_f32 v46, s80, v46\n"
"v_mul_f32 v47, s80, v47\n"
"v_cvt_pk_bf16_f32 v146, v44, v45\n"
"v_cvt_pk_bf16_f32 v147, v46, v47\n"
"ds_read_addtid_b32 v252 offset:256\n"
"v_pk_mul_f32 v[48:49], v[48:49], s[80:81]\n"
"v_pk_mul_f32 v[50:51], v[50:51], s[80:81]\n"
"v_cvt_pk_bf16_f32 v116, v48, v49\n"
"v_cvt_pk_bf16_f32 v117, v50, v51\n"
"v_pk_mul_f32 v[60:61], v[60:61], s[80:81]\n"
"v_pk_mul_f32 v[62:63], v[62:63], s[80:81]\n"
"v_cvt_pk_bf16_f32 v118, v60, v61\n"
"v_cvt_pk_bf16_f32 v119, v62, v63\n"
"v_pk_mul_f32 v[52:53], v[52:53], s[80:81]\n"
"v_pk_mul_f32 v[54:55], v[54:55], s[80:81]\n"
"v_cvt_pk_bf16_f32 v132, v52, v53\n"
"v_cvt_pk_bf16_f32 v133, v54, v55\n"
"v_pk_mul_f32 v[64:65], v[64:65], s[80:81]\n"
"v_pk_mul_f32 v[66:67], v[66:67], s[80:81]\n"
"v_cvt_pk_bf16_f32 v134, v64, v65\n"
"v_cvt_pk_bf16_f32 v135, v66, v67\n"
"v_pk_mul_f32 v[56:57], v[56:57], s[80:81]\n"
"v_pk_mul_f32 v[58:59], v[58:59], s[80:81]\n"
"v_cvt_pk_bf16_f32 v148, v56, v57\n"
"v_cvt_pk_bf16_f32 v149, v58, v59\n"
"v_pk_mul_f32 v[68:69], v[68:69], s[80:81]\n"
"v_pk_mul_f32 v[70:71], v[70:71], s[80:81]\n"
"v_cvt_pk_bf16_f32 v150, v68, v69\n"
"v_cvt_pk_bf16_f32 v151, v70, v71\n"
"s_waitcnt lgkmcnt(0)\n"
"v_cmp_gt_u32 vcc, v251, v252\n"
"s_cbranch_vccnz L_sp_pre\n"
"L_r_pre:\n"
"ds_read_b128 v[204:207], v248 offset:57344\n"
"ds_read_b128 v[208:211], v248 offset:57856\n"
"ds_read_b128 v[212:215], v248 offset:58368\n"
"ds_read_b128 v[216:219], v248 offset:58880\n"
"v_pk_mul_f32 v[72:73], v[72:73], s[80:81]\n"
"v_pk_mul_f32 v[74:75], v[74:75], s[80:81]\n"
"v_cvt_pk_bf16_f32 v120, v72, v73\n"
"v_cvt_pk_bf16_f32 v121, v74, v75\n"
"v_pk_mul_f32 v[84:85], v[84:85], s[80:81]\n"
"v_pk_mul_f32 v[86:87], v[86:87], s[80:81]\n"
"v_cvt_pk_bf16_f32 v122, v84, v85\n"
"v_cvt_pk_bf16_f32 v123, v86, v87\n"
"v_pk_mul_f32 v[76:77], v[76:77], s[80:81]\n"
"v_pk_mul_f32 v[78:79], v[78:79], s[80:81]\n"
"v_cvt_pk_bf16_f32 v136, v76, v77\n"
"v_cvt_pk_bf16_f32 v137, v78, v79\n"
"v_pk_mul_f32 v[88:89], v[88:89], s[80:81]\n"
"v_pk_mul_f32 v[90:91], v[90:91], s[80:81]\n"
"v_cvt_pk_bf16_f32 v138, v88, v89\n"
"v_cvt_pk_bf16_f32 v139, v90, v91\n"
"v_pk_mul_f32 v[80:81], v[80:81], s[80:81]\n"
"v_pk_mul_f32 v[82:83], v[82:83], s[80:81]\n"
"v_cvt_pk_bf16_f32 v152, v80, v81\n"
"v_cvt_pk_bf16_f32 v153, v82, v83\n"
"v_pk_mul_f32 v[92:93], v[92:93], s[80:81]\n"
"v_pk_mul_f32 v[94:95], v[94:95], s[80:81]\n"
"v_cvt_pk_bf16_f32 v154, v92, v93\n"
"v_cvt_pk_bf16_f32 v155, v94, v95\n"
"s_mov_b32 exec_lo, 0xffff0000\n"
"s_mov_b32 exec_hi, 0xffff0000\n"
"v_swap_b32 v108, v110\n"
"v_swap_b32 v109, v111\n"
"v_swap_b32 v112, v114\n"
"v_swap_b32 v113, v115\n"
"v_swap_b32 v116, v118\n"
"v_swap_b32 v117, v119\n"
"v_swap_b32 v120, v122\n"
"v_swap_b32 v121, v123\n"
"v_swap_b32 v124, v126\n"
"v_swap_b32 v125, v127\n"
"v_swap_b32 v128, v130\n"
"v_swap_b32 v129, v131\n"
"v_swap_b32 v132, v134\n"
"v_swap_b32 v133, v135\n"
"v_swap_b32 v136, v138\n"
"v_swap_b32 v137, v139\n"
"v_swap_b32 v140, v142\n"
"v_swap_b32 v141, v143\n"
"v_swap_b32 v144, v146\n"
"v_swap_b32 v145, v147\n"
"v_swap_b32 v148, v150\n"
"v_swap_b32 v149, v151\n"
"v_swap_b32 v152, v154\n"
"v_swap_b32 v153, v155\n"
"s_mov_b64 exec, -1\n"
"s_waitcnt lgkmcnt(3)\n"
"v_mfma_f32_16x16x32_bf16 v[180:183], v[204:207], v[108:111], 0\n"
"v_mov_b32 v192, 0\n"
"v_mov_b32 v193, 0\n"
"v_mov_b32 v194, 0\n"
"s_waitcnt lgkmcnt(2)\n"
"v_mfma_f32_16x16x32_bf16 v[180:183], v[208:211], v[112:115], v[180:183]\n"
"v_mov_b32 v195, 0\n"
"v_mov_b32 v196, 0\n"
"v_mov_b32 v197, 0\n"
"s_waitcnt lgkmcnt(1)\n"
"v_mfma_f32_16x16x32_bf16 v[180:183], v[212:215], v[116:119], v[180:183]\n"
"v_mov_b32 v198, 0\n"
"v_mov_b32 v199, 0\n"
"v_mov_b32 v200, 0\n"
"s_waitcnt lgkmcnt(0)\n"
"v_mfma_f32_16x16x32_bf16 v[180:183], v[216:219], v[120:123], v[180:183]\n"
"v_mov_b32 v201, 0\n"
"v_mov_b32 v202, 0\n"
"v_mov_b32 v203, 0\n"
"v_mfma_f32_16x16x32_bf16 v[184:187], v[204:207], v[124:127], 0\n"
"v_mov_b32 v160, 0\n"
"v_mov_b32 v161, 0\n"
"v_mov_b32 v162, 0\n"
"v_mfma_f32_16x16x32_bf16 v[184:187], v[208:211], v[128:131], v[184:187]\n"
"v_mov_b32 v163, 0\n"
"v_mfma_f32_16x16x32_bf16 v[184:187], v[212:215], v[132:135], v[184:187]\n"
"v_mfma_f32_16x16x32_bf16 v[184:187], v[216:219], v[136:139], v[184:187]\n"
"v_mfma_f32_16x16x32_bf16 v[188:191], v[204:207], v[140:143], 0\n"
"v_mfma_f32_16x16x32_bf16 v[188:191], v[208:211], v[144:147], v[188:191]\n"
"v_mfma_f32_16x16x32_bf16 v[188:191], v[212:215], v[148:151], v[188:191]\n"
"v_mfma_f32_16x16x32_bf16 v[188:191], v[216:219], v[152:155], v[188:191]\n"
"v_max3_f32 v180, v180, v181, v182\n"
"s_nop 2\n"
"v_max3_f32 v184, v184, v185, v186\n"
"s_nop 2\n"
"v_max3_f32 v188, v188, v189, v190\n"
"v_max3_f32 v180, v180, v183, v184\n"
"v_max3_f32 v187, v187, v188, v191\n"
"v_max_f32 v180, v180, v187\n"
"v_mov_b32 v181, v180\n"
"s_nop 1\n"
"v_permlane32_swap_b32 v180, v181\n"
"v_max_f32 v180, v180, v181\n"
"v_mov_b32 v181, v180\n"
"s_nop 1\n"
"v_permlane16_swap_b32 v180, v181\n"
"v_max_f32 v180, v180, v181\n"
"v_xor_b32 v156, 0x80000000, v180\n"
"v_xor_b32 v157, 0x80000000, v180\n"
"v_xor_b32 v158, 0x80000000, v180\n"
"v_xor_b32 v159, 0x80000000, v180\n"
"s_mov_b32 s66, 0\n"
"s_cbranch_vccnz L_sp_p0\n"
"L_r_p0:\n"
"v_mfma_f32_16x16x32_bf16 v[172:175], v[204:207], v[108:111], v[156:159]\n"
"v_mfma_f32_16x16x32_bf16 v[172:175], v[208:211], v[112:115], v[172:175]\n"
"v_mfma_f32_16x16x32_bf16 v[172:175], v[212:215], v[116:119], v[172:175]\n"
"v_mfma_f32_16x16x32_bf16 v[172:175], v[216:219], v[120:123], v[172:175]\n"
"v_mfma_f32_32x32x16_bf16 v[80:95], v[236:239], v[236:239], 0\n"
"ds_read_b128 v[220:223], v248 offset:49152\n"
"ds_read_b128 v[224:227], v248 offset:49664\n"
"ds_read_b128 v[228:231], v248 offset:50176\n"
"ds_read_b128 v[232:235], v248 offset:50688\n"
"v_mfma_f32_16x16x32_bf16 v[176:179], v[204:207], v[124:127], v[156:159]\n"
"s_nop 1\n"
"v_exp_f32 v172, v172\n"
"v_mfma_f32_16x16x32_bf16 v[176:179], v[208:211], v[128:131], v[176:179]\n"
"v_exp_f32 v173, v173\n"
"v_mfma_f32_16x16x32_bf16 v[176:179], v[212:215], v[132:135], v[176:179]\n"
"v_exp_f32 v174, v174\n"
"v_mfma_f32_16x16x32_bf16 v[176:179], v[216:219], v[136:139], v[176:179]\n"
"v_exp_f32 v175, v175\n"
"v_mfma_f32_32x32x16_bf16 v[64:79], v[236:239], v[236:239], 0\n"
"v_cvt_pk_bf16_f32 v180, v172, v173\n"
"v_cvt_pk_bf16_f32 v181, v174, v175\n"
"v_mfma_f32_16x16x32_bf16 v[172:175], v[204:207], v[140:143], v[156:159]\n"
"s_nop 2\n"
"v_exp_f32 v176, v176\n"
"v_mfma_f32_16x16x32_bf16 v[172:175], v[208:211], v[144:147], v[172:175]\n"
"v_exp_f32 v177, v177\n"
"v_mfma_f32_16x16x32_bf16 v[172:175], v[212:215], v[148:151], v[172:175]\n"
"v_exp_f32 v178, v178\n"
"v_mfma_f32_16x16x32_bf16 v[172:175], v[216:219], v[152:155], v[172:175]\n"
"v_exp_f32 v179, v179\n"
"v_mfma_f32_32x32x16_bf16 v[0:15], v[236:239], v[236:239], 0\n"
"v_cvt_pk_bf16_f32 v184, v176, v177\n"
"v_cvt_pk_bf16_f32 v185, v178, v179\n"
"ds_read_b128 v[204:207], v248 offset:53248\n"
"ds_read_b128 v[208:211], v248 offset:53760\n"
"v_add_u32 v251, 1, v251\n"
"s_waitcnt lgkmcnt(5)\n"
"v_mfma_f32_16x16x32_bf16 v[176:179], v[220:223], v[108:111], v[156:159]\n"
"v_exp_f32 v172, v172\n"
"s_waitcnt lgkmcnt(4)\n"
"v_mfma_f32_16x16x32_bf16 v[176:179], v[224:227], v[112:115], v[176:179]\n"
"v_exp_f32 v173, v173\n"
"s_waitcnt lgkmcnt(3)\n"
"v_mfma_f32_16x16x32_bf16 v[176:179], v[228:231], v[116:119], v[176:179]\n"
"v_exp_f32 v174, v174\n"
"s_waitcnt lgkmcnt(2)\n"
"v_mfma_f32_16x16x32_bf16 v[176:179], v[232:235], v[120:123], v[176:179]\n"
"v_exp_f32 v175, v175\n"
"v_mfma_f32_32x32x16_bf16 v[16:31], v[236:239], v[236:239], 0\n"
"v_cvt_pk_bf16_f32 v188, v172, v173\n"
"v_cvt_pk_bf16_f32 v189, v174, v175\n"
"ds_read_b128 v[212:215], v248 offset:54272\n"
"ds_read_b128 v[216:219], v248 offset:54784\n"
"v_mfma_f32_16x16x32_bf16 v[172:175], v[220:223], v[124:127], v[156:159]\n"
"s_nop 0\n"
"v_exp_f32 v176, v176\n"
"v_mfma_f32_16x16x32_bf16 v[172:175], v[224:227], v[128:131], v[172:175]\n"
"v_exp_f32 v177, v177\n"
"v_mfma_f32_16x16x32_bf16 v[172:175], v[228:231], v[132:135], v[172:175]\n"
"v_exp_f32 v178, v178\n"
"v_mfma_f32_16x16x32_bf16 v[172:175], v[232:235], v[136:139], v[172:175]\n"
"v_exp_f32 v179, v179\n"
"v_mfma_f32_32x32x16_bf16 v[32:47], v[236:239], v[236:239], 0\n"
"v_cvt_pk_bf16_f32 v182, v176, v177\n"
"v_cvt_pk_bf16_f32 v183, v178, v179\n"
"ds_read_addtid_b32 v252 offset:256\n"
"ds_write_addtid_b32 v251\n"
"v_mfma_f32_16x16x32_bf16 v[176:179], v[220:223], v[140:143], v[156:159]\n"
"s_nop 0\n"
"v_exp_f32 v172, v172\n"
"v_mfma_f32_16x16x32_bf16 v[176:179], v[224:227], v[144:147], v[176:179]\n"
"v_exp_f32 v173, v173\n"
"v_mfma_f32_16x16x32_bf16 v[176:179], v[228:231], v[148:151], v[176:179]\n"
"v_exp_f32 v174, v174\n"
"v_mfma_f32_16x16x32_bf16 v[176:179], v[232:235], v[152:155], v[176:179]\n"
"v_exp_f32 v175, v175\n"
"v_mfma_f32_32x32x16_bf16 v[48:63], v[236:239], v[236:239], 0\n"
"v_cvt_pk_bf16_f32 v186, v172, v173\n"
"v_cvt_pk_bf16_f32 v187, v174, v175\n"
"s_waitcnt lgkmcnt(1)\n"
"v_cmp_gt_u32 vcc, v251, v252\n"
"s_nop 3\n"
"s_branch L_it1\n"
"L_aloop:\n"
"s_cbranch_vccnz L_sp_a0\n"
"L_r_a0:\n"
"v_mfma_f32_16x16x32_bf16 v[172:175], v[204:207], v[108:111], v[156:159]\n"
"v_exp_f32 v176, v176\n"
"v_mfma_f32_16x16x32_bf16 v[172:175], v[208:211], v[112:115], v[172:175]\n"
"v_exp_f32 v177, v177\n"
"v_mfma_f32_16x16x32_bf16 v[172:175], v[212:215], v[116:119], v[172:175]\n"
"v_exp_f32 v178, v178\n"
"v_mfma_f32_16x16x32_bf16 v[172:175], v[216:219], v[120:123], v[172:175]\n"
"v_exp_f32 v179, v179\n"
"v_mfma_f32_16x16x32_bf16 v[72:75], v[244:247], v[180:183], v[72:75]\n"
"ds_read_b64_tr_b16 v[236:237], v249 offset:49152\n"
"ds_read_b64_tr_b16 v[238:239], v249 offset:53248\n"
"v_mfma_f32_16x16x32_bf16 v[76:79], v[244:247], v[184:187], v[76:79]\n"
"v_cvt_pk_bf16_f32 v202, v176, v177\n"
"v_cvt_pk_bf16_f32 v203, v178, v179\n"
"v_mfma_f32_16x16x32_bf16 v[80:83], v[244:247], v[188:191], v[80:83]\n"
"ds_read_b64_tr_b16 v[240:241], v250 offset:49152\n"
"ds_read_b64_tr_b16 v[242:243], v250 offset:53248\n"
"v_mfma_f32_16x16x32_bf16 v[84:87], v[160:163], v[180:183], v[84:87]\n"
"ds_read_b128 v[220:223], v248 offset:49152\n"
"ds_read_b128 v[224:227], v248 offset:49664\n"
"v_mfma_f32_16x16x32_bf16 v[88:91], v[160:163], v[184:187], v[88:91]\n"
"ds_read_b64_tr_b16 v[244:245], v249 offset:49664\n"
"ds_read_b64_tr_b16 v[246:247], v249 offset:53760\n"
"v_mfma_f32_16x16x32_bf16 v[92:95], v[160:163], v[188:191], v[92:95]\n"
"ds_read_b128 v[228:231], v248 offset:50176\n"
"ds_read_b128 v[232:235], v248 offset:50688\n"
"v_mfma_f32_16x16x32_bf16 v[176:179], v[204:207], v[124:127], v[156:159]\n"
"v_exp_f32 v172, v172\n"
"v_mfma_f32_16x16x32_bf16 v[176:179], v[208:211], v[128:131], v[176:179]\n"
"v_exp_f32 v173, v173\n"
"v_mfma_f32_16x16x32_bf16 v[176:179], v[212:215], v[132:135], v[176:179]\n"
"v_exp_f32 v174, v174\n"
"v_mfma_f32_16x16x32_bf16 v[176:179], v[216:219], v[136:139], v[176:179]\n"
"v_exp_f32 v175, v175\n"
"v_smfmac_f32_16x16x64_bf16 v[100:103], v[168:171], v[188:195], v254 abid:1\n"
"v_smfmac_f32_16x16x64_bf16 v[104:107], v[168:171], v[196:203], v254 abid:1\n"
"s_waitcnt lgkmcnt(2)\n"
"v_mfma_f32_16x16x32_bf16 v[0:3], v[236:239], v[192:195], v[0:3]\n"
"v_cvt_pk_bf16_f32 v180, v172, v173\n"
"v_cvt_pk_bf16_f32 v181, v174, v175\n"
"v_mfma_f32_16x16x32_bf16 v[4:7], v[236:239], v[196:199], v[4:7]\n"
"ds_read_b64_tr_b16 v[160:161], v250 offset:49664\n"
"ds_read_b64_tr_b16 v[162:163], v250 offset:53760\n"
"v_mfma_f32_16x16x32_bf16 v[8:11], v[236:239], v[200:203], v[8:11]\n"
"ds_read_b64_tr_b16 v[236:237], v249 offset:50176\n"
"ds_read_b64_tr_b16 v[238:239], v249 offset:54272\n"
"v_mfma_f32_16x16x32_bf16 v[172:175], v[204:207], v[140:143], v[156:159]\n"
"v_exp_f32 v176, v176\n"
"v_mfma_f32_16x16x32_bf16 v[172:175], v[208:211], v[144:147], v[172:175]\n"
"v_exp_f32 v177, v177\n"
"v_mfma_f32_16x16x32_bf16 v[172:175], v[212:215], v[148:151], v[172:175]\n"
"v_exp_f32 v178, v178\n"
"v_mfma_f32_16x16x32_bf16 v[172:175], v[216:219], v[152:155], v[172:175]\n"
"v_exp_f32 v179, v179\n"
"v_mfma_f32_16x16x32_bf16 v[12:15], v[240:243], v[192:195], v[12:15]\n"
"v_cvt_pk_bf16_f32 v184, v176, v177\n"
"v_cvt_pk_bf16_f32 v185, v178, v179\n"
"v_mfma_f32_16x16x32_bf16 v[16:19], v[240:243], v[196:199], v[16:19]\n"
"ds_read_b128 v[204:207], v248 offset:53248\n"
"ds_read_b128 v[208:211], v248 offset:53760\n"
"v_mfma_f32_16x16x32_bf16 v[20:23], v[240:243], v[200:203], v[20:23]\n"
"ds_read_b64_tr_b16 v[240:241], v250 offset:50176\n"
"ds_read_b64_tr_b16 v[242:243], v250 offset:54272\n"
"v_mfma_f32_16x16x32_bf16 v[24:27], v[244:247], v[192:195], v[24:27]\n"
"v_add_u32 v251, 1, v251\n"
"s_waitcnt lgkmcnt(4)\n"
"v_mfma_f32_16x16x32_bf16 v[176:179], v[220:223], v[108:111], v[156:159]\n"
"v_exp_f32 v172, v172\n"
"v_mfma_f32_16x16x32_bf16 v[176:179], v[224:227], v[112:115], v[176:179]\n"
"v_exp_f32 v173, v173\n"
"v_mfma_f32_16x16x32_bf16 v[176:179], v[228:231], v[116:119], v[176:179]\n"
"v_exp_f32 v174, v174\n"
"v_mfma_f32_16x16x32_bf16 v[176:179], v[232:235], v[120:123], v[176:179]\n"
"v_exp_f32 v175, v175\n"
"v_mfma_f32_16x16x32_bf16 v[28:31], v[244:247], v[196:199], v[28:31]\n"
"v_cvt_pk_bf16_f32 v188, v172, v173\n"
"v_cvt_pk_bf16_f32 v189, v174, v175\n"
"v_mfma_f32_16x16x32_bf16 v[32:35], v[244:247], v[200:203], v[32:35]\n"
"ds_read_b64_tr_b16 v[244:245], v249 offset:50688\n"
"ds_read_b64_tr_b16 v[246:247], v249 offset:54784\n"
"v_mfma_f32_16x16x32_bf16 v[36:39], v[160:163], v[192:195], v[36:39]\n"
"ds_read_b128 v[212:215], v248 offset:54272\n"
"ds_read_b128 v[216:219], v248 offset:54784\n"
"v_mfma_f32_16x16x32_bf16 v[40:43], v[160:163], v[196:199], v[40:43]\n"
"s_waitcnt lgkmcnt(4)\n"
"v_mfma_f32_16x16x32_bf16 v[172:175], v[220:223], v[124:127], v[156:159]\n"
"v_exp_f32 v176, v176\n"
"v_mfma_f32_16x16x32_bf16 v[172:175], v[224:227], v[128:131], v[172:175]\n"
"v_exp_f32 v177, v177\n"
"v_mfma_f32_16x16x32_bf16 v[172:175], v[228:231], v[132:135], v[172:175]\n"
"v_exp_f32 v178, v178\n"
"v_mfma_f32_16x16x32_bf16 v[172:175], v[232:235], v[136:139], v[172:175]\n"
"v_exp_f32 v179, v179\n"
"v_mfma_f32_16x16x32_bf16 v[44:47], v[160:163], v[200:203], v[44:47]\n"
"ds_read_b64_tr_b16 v[160:161], v250 offset:50688\n"
"ds_read_b64_tr_b16 v[162:163], v250 offset:54784\n"
"v_mfma_f32_16x16x32_bf16 v[48:51], v[236:239], v[192:195], v[48:51]\n"
"v_cvt_pk_bf16_f32 v182, v176, v177\n"
"v_cvt_pk_bf16_f32 v183, v178, v179\n"
"v_mfma_f32_16x16x32_bf16 v[52:55], v[236:239], v[196:199], v[52:55]\n"
"ds_read_addtid_b32 v252 offset:256\n"
"ds_write_addtid_b32 v251\n"
"v_mfma_f32_16x16x32_bf16 v[56:59], v[236:239], v[200:203], v[56:59]\n"
"v_mfma_f32_16x16x32_bf16 v[176:179], v[220:223], v[140:143], v[156:159]\n"
"v_exp_f32 v172, v172\n"
"v_mfma_f32_16x16x32_bf16 v[176:179], v[224:227], v[144:147], v[176:179]\n"
"v_exp_f32 v173, v173\n"
"v_mfma_f32_16x16x32_bf16 v[176:179], v[228:231], v[148:151], v[176:179]\n"
"v_exp_f32 v174, v174\n"
"v_mfma_f32_16x16x32_bf16 v[176:179], v[232:235], v[152:155], v[176:179]\n"
"v_exp_f32 v175, v175\n"
"v_mfma_f32_16x16x32_bf16 v[60:63], v[240:243], v[192:195], v[60:63]\n"
"v_cvt_pk_bf16_f32 v186, v172, v173\n"
"v_cvt_pk_bf16_f32 v187, v174, v175\n"
"v_mfma_f32_16x16x32_bf16 v[64:67], v[240:243], v[196:199], v[64:67]\n"
"s_waitcnt lgkmcnt(1)\n"
"v_cmp_gt_u32 vcc, v251, v252\n"
"v_mfma_f32_16x16x32_bf16 v[68:71], v[240:243], v[200:203], v[68:71]\n"
"L_it1:\n"
"s_cbranch_vccnz L_sp_a1\n"
"L_r_a1:\n"
"v_mfma_f32_16x16x32_bf16 v[172:175], v[204:207], v[108:111], v[156:159]\n"
"v_exp_f32 v176, v176\n"
"v_mfma_f32_16x16x32_bf16 v[172:175], v[208:211], v[112:115], v[172:175]\n"
"v_exp_f32 v177, v177\n"
"v_mfma_f32_16x16x32_bf16 v[172:175], v[212:215], v[116:119], v[172:175]\n"
"v_exp_f32 v178, v178\n"
"v_mfma_f32_16x16x32_bf16 v[172:175], v[216:219], v[120:123], v[172:175]\n"
"v_exp_f32 v179, v179\n"
"v_mfma_f32_16x16x32_bf16 v[72:75], v[244:247], v[192:195], v[72:75]\n"
"ds_read_b64_tr_b16 v[236:237], v249 offset:32768\n"
"ds_read_b64_tr_b16 v[238:239], v249 offset:36864\n"
"v_mfma_f32_16x16x32_bf16 v[76:79], v[244:247], v[196:199], v[76:79]\n"
"v_cvt_pk_bf16_f32 v190, v176, v177\n"
"v_cvt_pk_bf16_f32 v191, v178, v179\n"
"v_mfma_f32_16x16x32_bf16 v[80:83], v[244:247], v[200:203], v[80:83]\n"
"ds_read_b64_tr_b16 v[240:241], v250 offset:32768\n"
"ds_read_b64_tr_b16 v[242:243], v250 offset:36864\n"
"v_mfma_f32_16x16x32_bf16 v[84:87], v[160:163], v[192:195], v[84:87]\n"
"ds_read_b128 v[220:223], v248 offset:32768\n"
"ds_read_b128 v[224:227], v248 offset:33280\n"
"v_mfma_f32_16x16x32_bf16 v[88:91], v[160:163], v[196:199], v[88:91]\n"
"ds_read_b64_tr_b16 v[244:245], v249 offset:33280\n"
"ds_read_b64_tr_b16 v[246:247], v249 offset:37376\n"
"v_mfma_f32_16x16x32_bf16 v[92:95], v[160:163], v[200:203], v[92:95]\n"
"ds_read_b128 v[228:231], v248 offset:33792\n"
"ds_read_b128 v[232:235], v248 offset:34304\n"
"v_mfma_f32_16x16x32_bf16 v[176:179], v[204:207], v[124:127], v[156:159]\n"
"v_exp_f32 v172, v172\n"
"v_mfma_f32_16x16x32_bf16 v[176:179], v[208:211], v[128:131], v[176:179]\n"
"v_exp_f32 v173, v173\n"
"v_mfma_f32_16x16x32_bf16 v[176:179], v[212:215], v[132:135], v[176:179]\n"
"v_exp_f32 v174, v174\n"
"v_mfma_f32_16x16x32_bf16 v[176:179], v[216:219], v[136:139], v[176:179]\n"
"v_exp_f32 v175, v175\n"
"v_smfmac_f32_16x16x64_bf16 v[96:99], v[168:171], v[180:187], v254 abid:1\n"
"s_waitcnt lgkmcnt(2)\n"
"v_mfma_f32_16x16x32_bf16 v[0:3], v[236:239], v[180:183], v[0:3]\n"
"v_cvt_pk_bf16_f32 v192, v172, v173\n"
"v_cvt_pk_bf16_f32 v193, v174, v175\n"
"v_mfma_f32_16x16x32_bf16 v[4:7], v[236:239], v[184:187], v[4:7]\n"
"ds_read_b64_tr_b16 v[160:161], v250 offset:33280\n"
"ds_read_b64_tr_b16 v[162:163], v250 offset:37376\n"
"v_mfma_f32_16x16x32_bf16 v[8:11], v[236:239], v[188:191], v[8:11]\n"
"ds_read_b64_tr_b16 v[236:237], v249 offset:33792\n"
"ds_read_b64_tr_b16 v[238:239], v249 offset:37888\n"
"v_mfma_f32_16x16x32_bf16 v[172:175], v[204:207], v[140:143], v[156:159]\n"
"v_exp_f32 v176, v176\n"
"v_mfma_f32_16x16x32_bf16 v[172:175], v[208:211], v[144:147], v[172:175]\n"
"v_exp_f32 v177, v177\n"
"v_mfma_f32_16x16x32_bf16 v[172:175], v[212:215], v[148:151], v[172:175]\n"
"v_exp_f32 v178, v178\n"
"v_mfma_f32_16x16x32_bf16 v[172:175], v[216:219], v[152:155], v[172:175]\n"
"v_exp_f32 v179, v179\n"
"v_mfma_f32_16x16x32_bf16 v[12:15], v[240:243], v[180:183], v[12:15]\n"
"v_cvt_pk_bf16_f32 v196, v176, v177\n"
"v_cvt_pk_bf16_f32 v197, v178, v179\n"
"v_mfma_f32_16x16x32_bf16 v[16:19], v[240:243], v[184:187], v[16:19]\n"
"ds_read_b128 v[204:207], v248 offset:36864\n"
"ds_read_b128 v[208:211], v248 offset:37376\n"
"v_mfma_f32_16x16x32_bf16 v[20:23], v[240:243], v[188:191], v[20:23]\n"
"ds_read_b64_tr_b16 v[240:241], v250 offset:33792\n"
"ds_read_b64_tr_b16 v[242:243], v250 offset:37888\n"
"v_mfma_f32_16x16x32_bf16 v[24:27], v[244:247], v[180:183], v[24:27]\n"
"v_add_u32 v251, 1, v251\n"
"s_waitcnt lgkmcnt(4)\n"
"v_mfma_f32_16x16x32_bf16 v[176:179], v[220:223], v[108:111], v[156:159]\n"
"v_exp_f32 v172, v172\n"
"v_mfma_f32_16x16x32_bf16 v[176:179], v[224:227], v[112:115], v[176:179]\n"
"v_exp_f32 v173, v173\n"
"v_mfma_f32_16x16x32_bf16 v[176:179], v[228:231], v[116:119], v[176:179]\n"
"v_exp_f32 v174, v174\n"
"v_mfma_f32_16x16x32_bf16 v[176:179], v[232:235], v[120:123], v[176:179]\n"
"v_exp_f32 v175, v175\n"
"v_mfma_f32_16x16x32_bf16 v[28:31], v[244:247], v[184:187], v[28:31]\n"
"v_cvt_pk_bf16_f32 v200, v172, v173\n"
"v_cvt_pk_bf16_f32 v201, v174, v175\n"
"v_mfma_f32_16x16x32_bf16 v[32:35], v[244:247], v[188:191], v[32:35]\n"
"ds_read_b64_tr_b16 v[244:245], v249 offset:34304\n"
"ds_read_b64_tr_b16 v[246:247], v249 offset:38400\n"
"v_mfma_f32_16x16x32_bf16 v[36:39], v[160:163], v[180:183], v[36:39]\n"
"ds_read_b128 v[212:215], v248 offset:37888\n"
"ds_read_b128 v[216:219], v248 offset:38400\n"
"v_mfma_f32_16x16x32_bf16 v[40:43], v[160:163], v[184:187], v[40:43]\n"
"s_waitcnt lgkmcnt(4)\n"
"v_mfma_f32_16x16x32_bf16 v[172:175], v[220:223], v[124:127], v[156:159]\n"
"v_exp_f32 v176, v176\n"
"v_mfma_f32_16x16x32_bf16 v[172:175], v[224:227], v[128:131], v[172:175]\n"
"v_exp_f32 v177, v177\n"
"v_mfma_f32_16x16x32_bf16 v[172:175], v[228:231], v[132:135], v[172:175]\n"
"v_exp_f32 v178, v178\n"
"v_mfma_f32_16x16x32_bf16 v[172:175], v[232:235], v[136:139], v[172:175]\n"
"v_exp_f32 v179, v179\n"
"v_mfma_f32_16x16x32_bf16 v[44:47], v[160:163], v[188:191], v[44:47]\n"
"ds_read_b64_tr_b16 v[160:161], v250 offset:34304\n"
"ds_read_b64_tr_b16 v[162:163], v250 offset:38400\n"
"v_mfma_f32_16x16x32_bf16 v[48:51], v[236:239], v[180:183], v[48:51]\n"
"v_cvt_pk_bf16_f32 v194, v176, v177\n"
"v_cvt_pk_bf16_f32 v195, v178, v179\n"
"v_mfma_f32_16x16x32_bf16 v[52:55], v[236:239], v[184:187], v[52:55]\n"
"ds_read_addtid_b32 v252 offset:256\n"
"ds_write_addtid_b32 v251\n"
"v_mfma_f32_16x16x32_bf16 v[56:59], v[236:239], v[188:191], v[56:59]\n"
"v_mfma_f32_16x16x32_bf16 v[176:179], v[220:223], v[140:143], v[156:159]\n"
"v_exp_f32 v172, v172\n"
"v_mfma_f32_16x16x32_bf16 v[176:179], v[224:227], v[144:147], v[176:179]\n"
"v_exp_f32 v173, v173\n"
"v_mfma_f32_16x16x32_bf16 v[176:179], v[228:231], v[148:151], v[176:179]\n"
"v_exp_f32 v174, v174\n"
"v_mfma_f32_16x16x32_bf16 v[176:179], v[232:235], v[152:155], v[176:179]\n"
"v_exp_f32 v175, v175\n"
"v_mfma_f32_16x16x32_bf16 v[60:63], v[240:243], v[180:183], v[60:63]\n"
"v_cvt_pk_bf16_f32 v198, v172, v173\n"
"v_cvt_pk_bf16_f32 v199, v174, v175\n"
"v_mfma_f32_16x16x32_bf16 v[64:67], v[240:243], v[184:187], v[64:67]\n"
"s_waitcnt lgkmcnt(1)\n"
"v_cmp_gt_u32 vcc, v251, v252\n"
"v_mfma_f32_16x16x32_bf16 v[68:71], v[240:243], v[188:191], v[68:71]\n"
"s_cbranch_vccnz L_sp_a2\n"
"L_r_a2:\n"
"v_mfma_f32_16x16x32_bf16 v[172:175], v[204:207], v[108:111], v[156:159]\n"
"v_exp_f32 v176, v176\n"
"v_mfma_f32_16x16x32_bf16 v[172:175], v[208:211], v[112:115], v[172:175]\n"
"v_exp_f32 v177, v177\n"
"v_mfma_f32_16x16x32_bf16 v[172:175], v[212:215], v[116:119], v[172:175]\n"
"v_exp_f32 v178, v178\n"
"v_mfma_f32_16x16x32_bf16 v[172:175], v[216:219], v[120:123], v[172:175]\n"
"v_exp_f32 v179, v179\n"
"v_mfma_f32_16x16x32_bf16 v[72:75], v[244:247], v[180:183], v[72:75]\n"
"ds_read_b64_tr_b16 v[236:237], v249 offset:16384\n"
"ds_read_b64_tr_b16 v[238:239], v249 offset:20480\n"
"v_mfma_f32_16x16x32_bf16 v[76:79], v[244:247], v[184:187], v[76:79]\n"
"v_cvt_pk_bf16_f32 v202, v176, v177\n"
"v_cvt_pk_bf16_f32 v203, v178, v179\n"
"v_mfma_f32_16x16x32_bf16 v[80:83], v[244:247], v[188:191], v[80:83]\n"
"ds_read_b64_tr_b16 v[240:241], v250 offset:16384\n"
"ds_read_b64_tr_b16 v[242:243], v250 offset:20480\n"
"v_mfma_f32_16x16x32_bf16 v[84:87], v[160:163], v[180:183], v[84:87]\n"
"ds_read_b128 v[220:223], v248 offset:16384\n"
"ds_read_b128 v[224:227], v248 offset:16896\n"
"v_mfma_f32_16x16x32_bf16 v[88:91], v[160:163], v[184:187], v[88:91]\n"
"ds_read_b64_tr_b16 v[244:245], v249 offset:16896\n"
"ds_read_b64_tr_b16 v[246:247], v249 offset:20992\n"
"v_mfma_f32_16x16x32_bf16 v[92:95], v[160:163], v[188:191], v[92:95]\n"
"ds_read_b128 v[228:231], v248 offset:17408\n"
"ds_read_b128 v[232:235], v248 offset:17920\n"
"v_mfma_f32_16x16x32_bf16 v[176:179], v[204:207], v[124:127], v[156:159]\n"
"v_exp_f32 v172, v172\n"
"v_mfma_f32_16x16x32_bf16 v[176:179], v[208:211], v[128:131], v[176:179]\n"
"v_exp_f32 v173, v173\n"
"v_mfma_f32_16x16x32_bf16 v[176:179], v[212:215], v[132:135], v[176:179]\n"
"v_exp_f32 v174, v174\n"
"v_mfma_f32_16x16x32_bf16 v[176:179], v[216:219], v[136:139], v[176:179]\n"
"v_exp_f32 v175, v175\n"
"v_smfmac_f32_16x16x64_bf16 v[100:103], v[168:171], v[188:195], v254 abid:1\n"
"v_smfmac_f32_16x16x64_bf16 v[104:107], v[168:171], v[196:203], v254 abid:1\n"
"s_waitcnt lgkmcnt(2)\n"
"v_mfma_f32_16x16x32_bf16 v[0:3], v[236:239], v[192:195], v[0:3]\n"
"v_cvt_pk_bf16_f32 v180, v172, v173\n"
"v_cvt_pk_bf16_f32 v181, v174, v175\n"
"v_mfma_f32_16x16x32_bf16 v[4:7], v[236:239], v[196:199], v[4:7]\n"
"ds_read_b64_tr_b16 v[160:161], v250 offset:16896\n"
"ds_read_b64_tr_b16 v[162:163], v250 offset:20992\n"
"v_mfma_f32_16x16x32_bf16 v[8:11], v[236:239], v[200:203], v[8:11]\n"
"ds_read_b64_tr_b16 v[236:237], v249 offset:17408\n"
"ds_read_b64_tr_b16 v[238:239], v249 offset:21504\n"
"v_mfma_f32_16x16x32_bf16 v[172:175], v[204:207], v[140:143], v[156:159]\n"
"v_exp_f32 v176, v176\n"
"v_mfma_f32_16x16x32_bf16 v[172:175], v[208:211], v[144:147], v[172:175]\n"
"v_exp_f32 v177, v177\n"
"v_mfma_f32_16x16x32_bf16 v[172:175], v[212:215], v[148:151], v[172:175]\n"
"v_exp_f32 v178, v178\n"
"v_mfma_f32_16x16x32_bf16 v[172:175], v[216:219], v[152:155], v[172:175]\n"
"v_exp_f32 v179, v179\n"
"v_mfma_f32_16x16x32_bf16 v[12:15], v[240:243], v[192:195], v[12:15]\n"
"v_cvt_pk_bf16_f32 v184, v176, v177\n"
"v_cvt_pk_bf16_f32 v185, v178, v179\n"
"v_mfma_f32_16x16x32_bf16 v[16:19], v[240:243], v[196:199], v[16:19]\n"
"ds_read_b128 v[204:207], v248 offset:20480\n"
"ds_read_b128 v[208:211], v248 offset:20992\n"
"v_mfma_f32_16x16x32_bf16 v[20:23], v[240:243], v[200:203], v[20:23]\n"
"ds_read_b64_tr_b16 v[240:241], v250 offset:17408\n"
"ds_read_b64_tr_b16 v[242:243], v250 offset:21504\n"
"v_mfma_f32_16x16x32_bf16 v[24:27], v[244:247], v[192:195], v[24:27]\n"
"v_add_u32 v251, 1, v251\n"
"s_waitcnt lgkmcnt(4)\n"
"v_mfma_f32_16x16x32_bf16 v[176:179], v[220:223], v[108:111], v[156:159]\n"
"v_exp_f32 v172, v172\n"
"v_mfma_f32_16x16x32_bf16 v[176:179], v[224:227], v[112:115], v[176:179]\n"
"v_exp_f32 v173, v173\n"
"v_mfma_f32_16x16x32_bf16 v[176:179], v[228:231], v[116:119], v[176:179]\n"
"v_exp_f32 v174, v174\n"
"v_mfma_f32_16x16x32_bf16 v[176:179], v[232:235], v[120:123], v[176:179]\n"
"v_exp_f32 v175, v175\n"
"v_mfma_f32_16x16x32_bf16 v[28:31], v[244:247], v[196:199], v[28:31]\n"
"v_cvt_pk_bf16_f32 v188, v172, v173\n"
"v_cvt_pk_bf16_f32 v189, v174, v175\n"
"v_mfma_f32_16x16x32_bf16 v[32:35], v[244:247], v[200:203], v[32:35]\n"
"ds_read_b64_tr_b16 v[244:245], v249 offset:17920\n"
"ds_read_b64_tr_b16 v[246:247], v249 offset:22016\n"
"v_mfma_f32_16x16x32_bf16 v[36:39], v[160:163], v[192:195], v[36:39]\n"
"ds_read_b128 v[212:215], v248 offset:21504\n"
"ds_read_b128 v[216:219], v248 offset:22016\n"
"v_mfma_f32_16x16x32_bf16 v[40:43], v[160:163], v[196:199], v[40:43]\n"
"s_waitcnt lgkmcnt(4)\n"
"v_mfma_f32_16x16x32_bf16 v[172:175], v[220:223], v[124:127], v[156:159]\n"
"v_exp_f32 v176, v176\n"
"v_mfma_f32_16x16x32_bf16 v[172:175], v[224:227], v[128:131], v[172:175]\n"
"v_exp_f32 v177, v177\n"
"v_mfma_f32_16x16x32_bf16 v[172:175], v[228:231], v[132:135], v[172:175]\n"
"v_exp_f32 v178, v178\n"
"v_mfma_f32_16x16x32_bf16 v[172:175], v[232:235], v[136:139], v[172:175]\n"
"v_exp_f32 v179, v179\n"
"v_mfma_f32_16x16x32_bf16 v[44:47], v[160:163], v[200:203], v[44:47]\n"
"ds_read_b64_tr_b16 v[160:161], v250 offset:17920\n"
"ds_read_b64_tr_b16 v[162:163], v250 offset:22016\n"
"v_mfma_f32_16x16x32_bf16 v[48:51], v[236:239], v[192:195], v[48:51]\n"
"v_cvt_pk_bf16_f32 v182, v176, v177\n"
"v_cvt_pk_bf16_f32 v183, v178, v179\n"
"v_mfma_f32_16x16x32_bf16 v[52:55], v[236:239], v[196:199], v[52:55]\n"
"ds_read_addtid_b32 v252 offset:256\n"
"ds_write_addtid_b32 v251\n"
"v_mfma_f32_16x16x32_bf16 v[56:59], v[236:239], v[200:203], v[56:59]\n"
"v_mfma_f32_16x16x32_bf16 v[176:179], v[220:223], v[140:143], v[156:159]\n"
"v_exp_f32 v172, v172\n"
"v_mfma_f32_16x16x32_bf16 v[176:179], v[224:227], v[144:147], v[176:179]\n"
"v_exp_f32 v173, v173\n"
"v_mfma_f32_16x16x32_bf16 v[176:179], v[228:231], v[148:151], v[176:179]\n"
"v_exp_f32 v174, v174\n"
"v_mfma_f32_16x16x32_bf16 v[176:179], v[232:235], v[152:155], v[176:179]\n"
"v_exp_f32 v175, v175\n"
"v_mfma_f32_16x16x32_bf16 v[60:63], v[240:243], v[192:195], v[60:63]\n"
"v_cvt_pk_bf16_f32 v186, v172, v173\n"
"v_cvt_pk_bf16_f32 v187, v174, v175\n"
"v_mfma_f32_16x16x32_bf16 v[64:67], v[240:243], v[196:199], v[64:67]\n"
"s_waitcnt lgkmcnt(1)\n"
"v_cmp_gt_u32 vcc, v251, v252\n"
"v_mfma_f32_16x16x32_bf16 v[68:71], v[240:243], v[200:203], v[68:71]\n"
"s_cbranch_vccnz L_sp_a3\n"
"L_r_a3:\n"
"v_mfma_f32_16x16x32_bf16 v[172:175], v[204:207], v[108:111], v[156:159]\n"
"v_exp_f32 v176, v176\n"
"v_mfma_f32_16x16x32_bf16 v[172:175], v[208:211], v[112:115], v[172:175]\n"
"v_exp_f32 v177, v177\n"
"v_mfma_f32_16x16x32_bf16 v[172:175], v[212:215], v[116:119], v[172:175]\n"
"v_exp_f32 v178, v178\n"
"v_mfma_f32_16x16x32_bf16 v[172:175], v[216:219], v[120:123], v[172:175]\n"
"v_exp_f32 v179, v179\n"
"v_mfma_f32_16x16x32_bf16 v[72:75], v[244:247], v[192:195], v[72:75]\n"
"ds_read_b64_tr_b16 v[236:237], v249\n"
"ds_read_b64_tr_b16 v[238:239], v249 offset:4096\n"
"v_mfma_f32_16x16x32_bf16 v[76:79], v[244:247], v[196:199], v[76:79]\n"
"v_cvt_pk_bf16_f32 v190, v176, v177\n"
"v_cvt_pk_bf16_f32 v191, v178, v179\n"
"v_mfma_f32_16x16x32_bf16 v[80:83], v[244:247], v[200:203], v[80:83]\n"
"ds_read_b64_tr_b16 v[240:241], v250\n"
"ds_read_b64_tr_b16 v[242:243], v250 offset:4096\n"
"v_mfma_f32_16x16x32_bf16 v[84:87], v[160:163], v[192:195], v[84:87]\n"
"ds_read_b128 v[220:223], v248\n"
"ds_read_b128 v[224:227], v248 offset:512\n"
"v_mfma_f32_16x16x32_bf16 v[88:91], v[160:163], v[196:199], v[88:91]\n"
"ds_read_b64_tr_b16 v[244:245], v249 offset:512\n"
"ds_read_b64_tr_b16 v[246:247], v249 offset:4608\n"
"v_mfma_f32_16x16x32_bf16 v[92:95], v[160:163], v[200:203], v[92:95]\n"
"ds_read_b128 v[228:231], v248 offset:1024\n"
"ds_read_b128 v[232:235], v248 offset:1536\n"
"v_mfma_f32_16x16x32_bf16 v[176:179], v[204:207], v[124:127], v[156:159]\n"
"v_exp_f32 v172, v172\n"
"v_mfma_f32_16x16x32_bf16 v[176:179], v[208:211], v[128:131], v[176:179]\n"
"v_exp_f32 v173, v173\n"
"v_mfma_f32_16x16x32_bf16 v[176:179], v[212:215], v[132:135], v[176:179]\n"
"v_exp_f32 v174, v174\n"
"v_mfma_f32_16x16x32_bf16 v[176:179], v[216:219], v[136:139], v[176:179]\n"
"v_exp_f32 v175, v175\n"
"v_smfmac_f32_16x16x64_bf16 v[96:99], v[168:171], v[180:187], v254 abid:1\n"
"s_waitcnt lgkmcnt(2)\n"
"v_mfma_f32_16x16x32_bf16 v[0:3], v[236:239], v[180:183], v[0:3]\n"
"v_cvt_pk_bf16_f32 v192, v172, v173\n"
"v_cvt_pk_bf16_f32 v193, v174, v175\n"
"v_mfma_f32_16x16x32_bf16 v[4:7], v[236:239], v[184:187], v[4:7]\n"
"ds_read_b64_tr_b16 v[160:161], v250 offset:512\n"
"ds_read_b64_tr_b16 v[162:163], v250 offset:4608\n"
"v_mfma_f32_16x16x32_bf16 v[8:11], v[236:239], v[188:191], v[8:11]\n"
"ds_read_b64_tr_b16 v[236:237], v249 offset:1024\n"
"ds_read_b64_tr_b16 v[238:239], v249 offset:5120\n"
"v_mfma_f32_16x16x32_bf16 v[172:175], v[204:207], v[140:143], v[156:159]\n"
"v_exp_f32 v176, v176\n"
"v_mfma_f32_16x16x32_bf16 v[172:175], v[208:211], v[144:147], v[172:175]\n"
"v_exp_f32 v177, v177\n"
"v_mfma_f32_16x16x32_bf16 v[172:175], v[212:215], v[148:151], v[172:175]\n"
"v_exp_f32 v178, v178\n"
"v_mfma_f32_16x16x32_bf16 v[172:175], v[216:219], v[152:155], v[172:175]\n"
"v_exp_f32 v179, v179\n"
"v_mfma_f32_16x16x32_bf16 v[12:15], v[240:243], v[180:183], v[12:15]\n"
"v_cvt_pk_bf16_f32 v196, v176, v177\n"
"v_cvt_pk_bf16_f32 v197, v178, v179\n"
"v_mfma_f32_16x16x32_bf16 v[16:19], v[240:243], v[184:187], v[16:19]\n"
"ds_read_b128 v[204:207], v248 offset:4096\n"
"ds_read_b128 v[208:211], v248 offset:4608\n"
"v_mfma_f32_16x16x32_bf16 v[20:23], v[240:243], v[188:191], v[20:23]\n"
"ds_read_b64_tr_b16 v[240:241], v250 offset:1024\n"
"ds_read_b64_tr_b16 v[242:243], v250 offset:5120\n"
"v_mfma_f32_16x16x32_bf16 v[24:27], v[244:247], v[180:183], v[24:27]\n"
"v_add_u32 v251, 1, v251\n"
"s_waitcnt lgkmcnt(4)\n"
"v_mfma_f32_16x16x32_bf16 v[176:179], v[220:223], v[108:111], v[156:159]\n"
"v_exp_f32 v172, v172\n"
"v_mfma_f32_16x16x32_bf16 v[176:179], v[224:227], v[112:115], v[176:179]\n"
"v_exp_f32 v173, v173\n"
"v_mfma_f32_16x16x32_bf16 v[176:179], v[228:231], v[116:119], v[176:179]\n"
"v_exp_f32 v174, v174\n"
"v_mfma_f32_16x16x32_bf16 v[176:179], v[232:235], v[120:123], v[176:179]\n"
"v_exp_f32 v175, v175\n"
"v_mfma_f32_16x16x32_bf16 v[28:31], v[244:247], v[184:187], v[28:31]\n"
"v_cvt_pk_bf16_f32 v200, v172, v173\n"
"v_cvt_pk_bf16_f32 v201, v174, v175\n"
"v_mfma_f32_16x16x32_bf16 v[32:35], v[244:247], v[188:191], v[32:35]\n"
"ds_read_b64_tr_b16 v[244:245], v249 offset:1536\n"
"ds_read_b64_tr_b16 v[246:247], v249 offset:5632\n"
"v_mfma_f32_16x16x32_bf16 v[36:39], v[160:163], v[180:183], v[36:39]\n"
"ds_read_b128 v[212:215], v248 offset:5120\n"
"ds_read_b128 v[216:219], v248 offset:5632\n"
"v_mfma_f32_16x16x32_bf16 v[40:43], v[160:163], v[184:187], v[40:43]\n"
"s_waitcnt lgkmcnt(4)\n"
"v_xor_b32 v248, 0x10000, v248\n"
"v_mfma_f32_16x16x32_bf16 v[172:175], v[220:223], v[124:127], v[156:159]\n"
"v_exp_f32 v176, v176\n"
"v_mfma_f32_16x16x32_bf16 v[172:175], v[224:227], v[128:131], v[172:175]\n"
"v_exp_f32 v177, v177\n"
"v_mfma_f32_16x16x32_bf16 v[172:175], v[228:231], v[132:135], v[172:175]\n"
"v_exp_f32 v178, v178\n"
"v_mfma_f32_16x16x32_bf16 v[172:175], v[232:235], v[136:139], v[172:175]\n"
"v_exp_f32 v179, v179\n"
"v_mfma_f32_16x16x32_bf16 v[44:47], v[160:163], v[188:191], v[44:47]\n"
"ds_read_b64_tr_b16 v[160:161], v250 offset:1536\n"
"ds_read_b64_tr_b16 v[162:163], v250 offset:5632\n"
"v_mfma_f32_16x16x32_bf16 v[48:51], v[236:239], v[180:183], v[48:51]\n"
"v_cvt_pk_bf16_f32 v194, v176, v177\n"
"v_cvt_pk_bf16_f32 v195, v178, v179\n"
"v_mfma_f32_16x16x32_bf16 v[52:55], v[236:239], v[184:187], v[52:55]\n"
"ds_read_addtid_b32 v252 offset:256\n"
"ds_write_addtid_b32 v251\n"
"v_mfma_f32_16x16x32_bf16 v[56:59], v[236:239], v[188:191], v[56:59]\n"
"v_xor_b32 v249, 0x10000, v249\n"
"v_xor_b32 v250, 0x10000, v250\n"
"v_mfma_f32_16x16x32_bf16 v[176:179], v[220:223], v[140:143], v[156:159]\n"
"v_exp_f32 v172, v172\n"
"v_mfma_f32_16x16x32_bf16 v[176:179], v[224:227], v[144:147], v[176:179]\n"
"v_exp_f32 v173, v173\n"
"v_mfma_f32_16x16x32_bf16 v[176:179], v[228:231], v[148:151], v[176:179]\n"
"v_exp_f32 v174, v174\n"
"v_mfma_f32_16x16x32_bf16 v[176:179], v[232:235], v[152:155], v[176:179]\n"
"v_exp_f32 v175, v175\n"
"v_mfma_f32_16x16x32_bf16 v[60:63], v[240:243], v[180:183], v[60:63]\n"
"v_cvt_pk_bf16_f32 v198, v172, v173\n"
"v_cvt_pk_bf16_f32 v199, v174, v175\n"
"v_mfma_f32_16x16x32_bf16 v[64:67], v[240:243], v[184:187], v[64:67]\n"
"s_waitcnt lgkmcnt(1)\n"
"v_cmp_gt_u32 vcc, v251, v252\n"
"v_mfma_f32_16x16x32_bf16 v[68:71], v[240:243], v[188:191], v[68:71]\n"
"s_add_u32 s66, s66, 1\n"
"s_cmp_lt_u32 s66, 8\n"
"s_cbranch_scc1 L_aloop\n"
"s_setprio 3\n"
"v_and_b32 v249, 0x3fff, v249\n"
"v_and_b32 v250, 0x3fff, v250\n"
"s_cbranch_vccnz L_sp_last\n"
"L_r_last:\n"
"ds_read_b64_tr_b16 v[204:205], v249\n"
"ds_read_b64_tr_b16 v[206:207], v249 offset:4096\n"
"v_exp_f32 v176, v176\n"
"v_mfma_f32_16x16x32_bf16 v[72:75], v[244:247], v[180:183], v[72:75]\n"
"ds_read_b64_tr_b16 v[208:209], v250\n"
"ds_read_b64_tr_b16 v[210:211], v250 offset:4096\n"
"v_exp_f32 v177, v177\n"
"v_mfma_f32_16x16x32_bf16 v[76:79], v[244:247], v[184:187], v[76:79]\n"
"ds_read_b64_tr_b16 v[212:213], v249 offset:512\n"
"ds_read_b64_tr_b16 v[214:215], v249 offset:4608\n"
"v_exp_f32 v178, v178\n"
"v_mfma_f32_16x16x32_bf16 v[80:83], v[244:247], v[188:191], v[80:83]\n"
"ds_read_b64_tr_b16 v[216:217], v250 offset:512\n"
"ds_read_b64_tr_b16 v[218:219], v250 offset:4608\n"
"v_exp_f32 v179, v179\n"
"v_mfma_f32_16x16x32_bf16 v[84:87], v[160:163], v[180:183], v[84:87]\n"
"ds_read_b64_tr_b16 v[220:221], v249 offset:1024\n"
"ds_read_b64_tr_b16 v[222:223], v249 offset:5120\n"
"v_mfma_f32_16x16x32_bf16 v[88:91], v[160:163], v[184:187], v[88:91]\n"
"ds_read_b64_tr_b16 v[224:225], v250 offset:1024\n"
"ds_read_b64_tr_b16 v[226:227], v250 offset:5120\n"
"v_mfma_f32_16x16x32_bf16 v[92:95], v[160:163], v[188:191], v[92:95]\n"
"v_smfmac_f32_16x16x64_bf16 v[100:103], v[168:171], v[188:195], v254 abid:1\n"
"v_cvt_pk_bf16_f32 v202, v176, v177\n"
"v_cvt_pk_bf16_f32 v203, v178, v179\n"
"v_add_f32 v96, v96, v97\n"
"v_add_f32 v98, v98, v99\n"
"v_smfmac_f32_16x16x64_bf16 v[104:107], v[168:171], v[196:203], v254 abid:1\n"
"ds_read_b64_tr_b16 v[228:229], v249 offset:1536\n"
"ds_read_b64_tr_b16 v[230:231], v249 offset:5632\n"
"s_waitcnt lgkmcnt(12)\n"
"v_mfma_f32_16x16x32_bf16 v[0:3], v[204:207], v[192:195], v[0:3]\n"
"ds_read_b64_tr_b16 v[232:233], v250 offset:1536\n"
"ds_read_b64_tr_b16 v[234:235], v250 offset:5632\n"
"v_add_f32 v101, v100, v101\n"
"s_waitcnt lgkmcnt(12)\n"
"v_mfma_f32_16x16x32_bf16 v[12:15], v[208:211], v[192:195], v[12:15]\n"
"v_add_f32 v102, v102, v103\n"
"v_mfma_f32_16x16x32_bf16 v[4:7], v[204:207], v[196:199], v[4:7]\n"
"v_add_f32 v96, v96, v102\n"
"v_mfma_f32_16x16x32_bf16 v[16:19], v[208:211], v[196:199], v[16:19]\n"
"v_add_f32 v105, v104, v105\n"
"v_add_f32 v106, v106, v107\n"
"v_and_b32 v254, 0xffff, v254\n"
"v_mfma_f32_16x16x32_bf16 v[8:11], v[204:207], v[200:203], v[8:11]\n"
"v_add_f32 v100, v98, v105\n"
"v_rcp_f32 v98, v96\n"
"v_mfma_f32_16x16x32_bf16 v[20:23], v[208:211], v[200:203], v[20:23]\n"
"v_add_f32 v104, v101, v106\n"
"v_rcp_f32 v102, v100\n"
"s_waitcnt lgkmcnt(10)\n"
"v_mfma_f32_16x16x32_bf16 v[24:27], v[212:215], v[192:195], v[24:27]\n"
"v_rcp_f32 v106, v104\n"
"s_waitcnt lgkmcnt(8)\n"
"v_mfma_f32_16x16x32_bf16 v[36:39], v[216:219], v[192:195], v[36:39]\n"
"v_min3_u32 v97, v96, v100, v104\n"
"v_max3_u32 v101, v96, v100, v104\n"
"v_cmp_gt_u32 vcc, 0x1f800000, v97\n"
"v_mfma_f32_16x16x32_bf16 v[28:31], v[212:215], v[196:199], v[28:31]\n"
"s_mov_b64 s[82:83], vcc\n"
"v_cmp_lt_u32 vcc, 0x5f800000, v101\n"
"v_mfma_f32_16x16x32_bf16 v[40:43], v[216:219], v[196:199], v[40:43]\n"
"s_or_b64 s[82:83], s[82:83], vcc\n"
"s_cbranch_scc1 L_exit\n"
"v_mfma_f32_16x16x32_bf16 v[32:35], v[212:215], v[200:203], v[32:35]\n"
"v_mul_f32 v0, v98, v0\n"
"v_mul_f32 v1, v98, v1\n"
"v_mul_f32 v2, v98, v2\n"
"v_mfma_f32_16x16x32_bf16 v[44:47], v[216:219], v[200:203], v[44:47]\n"
"v_mul_f32 v3, v98, v3\n"
"v_cvt_pk_bf16_f32 v0, v0, v1\n"
"v_cvt_pk_bf16_f32 v1, v2, v3\n"
"s_waitcnt lgkmcnt(6)\n"
"v_mfma_f32_16x16x32_bf16 v[48:51], v[220:223], v[192:195], v[48:51]\n"
"v_mul_f32 v12, v98, v12\n"
"v_mul_f32 v13, v98, v13\n"
"v_mul_f32 v14, v98, v14\n"
"s_waitcnt lgkmcnt(4)\n"
"v_mfma_f32_16x16x32_bf16 v[60:63], v[224:227], v[192:195], v[60:63]\n"
"v_mul_f32 v15, v98, v15\n"
"v_cvt_pk_bf16_f32 v2, v12, v13\n"
"v_cvt_pk_bf16_f32 v3, v14, v15\n"
"v_mfma_f32_16x16x32_bf16 v[52:55], v[220:223], v[196:199], v[52:55]\n"
"buffer_store_dwordx4 v[0:3], v254, s[72:75], s64 offen sc1\n"
"v_mul_f32 v4, v102, v4\n"
"v_mul_f32 v5, v102, v5\n"
"v_mfma_f32_16x16x32_bf16 v[64:67], v[224:227], v[196:199], v[64:67]\n"
"v_mul_f32 v6, v102, v6\n"
"v_mul_f32 v7, v102, v7\n"
"v_cvt_pk_bf16_f32 v4, v4, v5\n"
"v_mfma_f32_16x16x32_bf16 v[56:59], v[220:223], v[200:203], v[56:59]\n"
"v_cvt_pk_bf16_f32 v5, v6, v7\n"
"v_mul_f32 v16, v102, v16\n"
"v_mul_f32 v17, v102, v17\n"
"v_mfma_f32_16x16x32_bf16 v[68:71], v[224:227], v[200:203], v[68:71]\n"
"v_mul_f32 v18, v102, v18\n"
"v_mul_f32 v19, v102, v19\n"
"v_cvt_pk_bf16_f32 v6, v16, v17\n"
"s_waitcnt lgkmcnt(2)\n"
"v_mfma_f32_16x16x32_bf16 v[72:75], v[228:231], v[192:195], v[72:75]\n"
"v_cvt_pk_bf16_f32 v7, v18, v19\n"
"buffer_store_dwordx4 v[4:7], v254, s[72:75], s64 offen offset:1024 sc1\n"
"v_mul_f32 v8, v106, v8\n"
"s_waitcnt lgkmcnt(0)\n"
"v_mfma_f32_16x16x32_bf16 v[84:87], v[232:235], v[192:195], v[84:87]\n"
"v_mul_f32 v9, v106, v9\n"
"v_mul_f32 v10, v106, v10\n"
"v_mul_f32 v11, v106, v11\n"
"v_mfma_f32_16x16x32_bf16 v[76:79], v[228:231], v[196:199], v[76:79]\n"
"v_cvt_pk_bf16_f32 v8, v8, v9\n"
"v_cvt_pk_bf16_f32 v9, v10, v11\n"
"v_mul_f32 v20, v106, v20\n"
"v_mfma_f32_16x16x32_bf16 v[88:91], v[232:235], v[196:199], v[88:91]\n"
"v_mul_f32 v21, v106, v21\n"
"v_mul_f32 v22, v106, v22\n"
"v_mul_f32 v23, v106, v23\n"
"v_mfma_f32_16x16x32_bf16 v[80:83], v[228:231], v[200:203], v[80:83]\n"
"v_cvt_pk_bf16_f32 v10, v20, v21\n"
"v_cvt_pk_bf16_f32 v11, v22, v23\n"
"buffer_store_dwordx4 v[8:11], v254, s[72:75], s64 offen offset:2048 sc1\n"
"v_mfma_f32_16x16x32_bf16 v[92:95], v[232:235], v[200:203], v[92:95]\n"
"s_add_u32 s64, s64, 3072\n"
"v_mul_f32 v24, v98, v24\n"
"v_mul_f32 v25, v98, v25\n"
"v_mul_f32 v26, v98, v26\n"
"v_mul_f32 v27, v98, v27\n"
"v_cvt_pk_bf16_f32 v24, v24, v25\n"
"v_cvt_pk_bf16_f32 v25, v26, v27\n"
"v_mul_f32 v36, v98, v36\n"
"v_mul_f32 v37, v98, v37\n"
"v_mul_f32 v38, v98, v38\n"
"v_mul_f32 v39, v98, v39\n"
"v_cvt_pk_bf16_f32 v26, v36, v37\n"
"v_cvt_pk_bf16_f32 v27, v38, v39\n"
"buffer_store_dwordx4 v[24:27], v254, s[72:75], s64 offen sc1\n"
"v_pk_mul_f32 v[28:29], v[28:29], v[102:103] op_sel_hi:[1,0]\n"
"v_pk_mul_f32 v[30:31], v[30:31], v[102:103] op_sel_hi:[1,0]\n"
"v_cvt_pk_bf16_f32 v28, v28, v29\n"
"v_cvt_pk_bf16_f32 v29, v30, v31\n"
"v_pk_mul_f32 v[40:41], v[40:41], v[102:103] op_sel_hi:[1,0]\n"
"v_pk_mul_f32 v[42:43], v[42:43], v[102:103] op_sel_hi:[1,0]\n"
"v_cvt_pk_bf16_f32 v30, v40, v41\n"
"v_cvt_pk_bf16_f32 v31, v42, v43\n"
"buffer_store_dwordx4 v[28:31], v254, s[72:75], s64 offen offset:1024 sc1\n"
"v_pk_mul_f32 v[32:33], v[32:33], v[106:107] op_sel_hi:[1,0]\n"
"v_pk_mul_f32 v[34:35], v[34:35], v[106:107] op_sel_hi:[1,0]\n"
"v_cvt_pk_bf16_f32 v32, v32, v33\n"
"v_cvt_pk_bf16_f32 v33, v34, v35\n"
"v_pk_mul_f32 v[44:45], v[44:45], v[106:107] op_sel_hi:[1,0]\n"
"v_pk_mul_f32 v[46:47], v[46:47], v[106:107] op_sel_hi:[1,0]\n"
"v_cvt_pk_bf16_f32 v34, v44, v45\n"
"v_cvt_pk_bf16_f32 v35, v46, v47\n"
"buffer_store_dwordx4 v[32:35], v254, s[72:75], s64 offen offset:2048 sc1\n"
"s_add_u32 s64, s64, 3072\n"
"v_pk_mul_f32 v[48:49], v[48:49], v[98:99] op_sel_hi:[1,0]\n"
"v_pk_mul_f32 v[50:51], v[50:51], v[98:99] op_sel_hi:[1,0]\n"
"v_cvt_pk_bf16_f32 v48, v48, v49\n"
"v_cvt_pk_bf16_f32 v49, v50, v51\n"
"v_pk_mul_f32 v[60:61], v[60:61], v[98:99] op_sel_hi:[1,0]\n"
"v_pk_mul_f32 v[62:63], v[62:63], v[98:99] op_sel_hi:[1,0]\n"
"v_cvt_pk_bf16_f32 v50, v60, v61\n"
"v_cvt_pk_bf16_f32 v51, v62, v63\n"
"buffer_store_dwordx4 v[48:51], v254, s[72:75], s64 offen sc1\n"
"v_pk_mul_f32 v[52:53], v[52:53], v[102:103] op_sel_hi:[1,0]\n"
"v_pk_mul_f32 v[54:55], v[54:55], v[102:103] op_sel_hi:[1,0]\n"
"v_cvt_pk_bf16_f32 v52, v52, v53\n"
"v_cvt_pk_bf16_f32 v53, v54, v55\n"
"v_pk_mul_f32 v[64:65], v[64:65], v[102:103] op_sel_hi:[1,0]\n"
"v_pk_mul_f32 v[66:67], v[66:67], v[102:103] op_sel_hi:[1,0]\n"
"v_cvt_pk_bf16_f32 v54, v64, v65\n"
"v_cvt_pk_bf16_f32 v55, v66, v67\n"
"buffer_store_dwordx4 v[52:55], v254, s[72:75], s64 offen offset:1024 sc1\n"
"v_pk_mul_f32 v[56:57], v[56:57], v[106:107] op_sel_hi:[1,0]\n"
"v_pk_mul_f32 v[58:59], v[58:59], v[106:107] op_sel_hi:[1,0]\n"
"v_cvt_pk_bf16_f32 v56, v56, v57\n"
"v_cvt_pk_bf16_f32 v57, v58, v59\n"
"v_pk_mul_f32 v[68:69], v[68:69], v[106:107] op_sel_hi:[1,0]\n"
"v_pk_mul_f32 v[70:71], v[70:71], v[106:107] op_sel_hi:[1,0]\n"
"v_cvt_pk_bf16_f32 v58, v68, v69\n"
"v_cvt_pk_bf16_f32 v59, v70, v71\n"
"buffer_store_dwordx4 v[56:59], v254, s[72:75], s64 offen offset:2048 sc1\n"
"s_add_u32 s64, s64, 3072\n"
"v_pk_mul_f32 v[72:73], v[72:73], v[98:99] op_sel_hi:[1,0]\n"
"v_pk_mul_f32 v[74:75], v[74:75], v[98:99] op_sel_hi:[1,0]\n"
"v_cvt_pk_bf16_f32 v72, v72, v73\n"
"v_cvt_pk_bf16_f32 v73, v74, v75\n"
"v_pk_mul_f32 v[84:85], v[84:85], v[98:99] op_sel_hi:[1,0]\n"
"v_pk_mul_f32 v[86:87], v[86:87], v[98:99] op_sel_hi:[1,0]\n"
"v_cvt_pk_bf16_f32 v74, v84, v85\n"
"v_cvt_pk_bf16_f32 v75, v86, v87\n"
"buffer_store_dwordx4 v[72:75], v254, s[72:75], s64 offen sc1\n"
"v_pk_mul_f32 v[76:77], v[76:77], v[102:103] op_sel_hi:[1,0]\n"
"v_pk_mul_f32 v[78:79], v[78:79], v[102:103] op_sel_hi:[1,0]\n"
"v_cvt_pk_bf16_f32 v76, v76, v77\n"
"v_cvt_pk_bf16_f32 v77, v78, v79\n"
"v_pk_mul_f32 v[88:89], v[88:89], v[102:103] op_sel_hi:[1,0]\n"
"v_pk_mul_f32 v[90:91], v[90:91], v[102:103] op_sel_hi:[1,0]\n"
"v_cvt_pk_bf16_f32 v78, v88, v89\n"
"v_cvt_pk_bf16_f32 v79, v90, v91\n"
"buffer_store_dwordx4 v[76:79], v254, s[72:75], s64 offen offset:1024 sc1\n"
"v_pk_mul_f32 v[80:81], v[80:81], v[106:107] op_sel_hi:[1,0]\n"
"v_pk_mul_f32 v[82:83], v[82:83], v[106:107] op_sel_hi:[1,0]\n"
"v_cvt_pk_bf16_f32 v80, v80, v81\n"
"v_cvt_pk_bf16_f32 v81, v82, v83\n"
"v_pk_mul_f32 v[92:93], v[92:93], v[106:107] op_sel_hi:[1,0]\n"
"v_pk_mul_f32 v[94:95], v[94:95], v[106:107] op_sel_hi:[1,0]\n"
"v_cvt_pk_bf16_f32 v82, v92, v93\n"
"v_cvt_pk_bf16_f32 v83, v94, v95\n"
"buffer_store_dwordx4 v[80:83], v254, s[72:75], s64 offen offset:2048 sc1\n"
"L_pretail:\n"
"ds_read_addtid_b32 v250 offset:2048\n"
"ds_read_addtid_b32 v251 offset:2304\n"
"ds_read_addtid_b32 v252 offset:256\n"
"v_mov_b32 v248, 41\n"
"s_mov_b64 s[68:69], s[44:45]\n"
"s_mov_b32 s70, 0x400000\n"
"s_mov_b32 s71, 0x20000\n"
"v_lshrrev_b32 v240, 3, v254\n"
"v_and_b32 v240, 0x60, v240\n"
"v_and_b32 v241, 0x30, v254\n"
"v_xor_b32 v240, v240, v241\n"
"v_and_b32 v241, 0x1c0, v254\n"
"v_lshl_or_b32 v244, v241, 4, v240\n"
"v_xor_b32 v245, 48, v244\n"
"v_add_u32 v245, 7168, v245\n"
"v_add_u32 v246, 0x10000, v255\n"
"v_xor_b32 v247, 32, v246\n"
"s_waitcnt lgkmcnt(0)\n"
"v_cmp_gt_u32 vcc, 41, v252\n"
"s_cbranch_vccnz L_sp_w0\n"
"L_r_w:\n"
"v_xor_b32 v252, 32, v255\n"
"ds_read_b64_tr_b16 v[48:49], v246\n"
"ds_read_b64_tr_b16 v[50:51], v246 offset:2048\n"
"ds_read_b64_tr_b16 v[52:53], v247\n"
"ds_read_b64_tr_b16 v[54:55], v247 offset:2048\n"
"ds_read_b64_tr_b16 v[56:57], v246 offset:512\n"
"ds_read_b64_tr_b16 v[58:59], v246 offset:2560\n"
"ds_read_b64_tr_b16 v[60:61], v247 offset:512\n"
"ds_read_b64_tr_b16 v[62:63], v247 offset:2560\n"
"ds_read_b64_tr_b16 v[64:65], v255 offset:49152\n"
"ds_read_b64_tr_b16 v[66:67], v255 offset:51200\n"
"ds_read_b64_tr_b16 v[68:69], v252 offset:49152\n"
"ds_read_b64_tr_b16 v[70:71], v252 offset:51200\n"
"ds_read_b64_tr_b16 v[72:73], v255 offset:49664\n"
"ds_read_b64_tr_b16 v[74:75], v255 offset:51712\n"
"ds_read_b64_tr_b16 v[76:77], v252 offset:49664\n"
"ds_read_b64_tr_b16 v[78:79], v252 offset:51712\n"
"s_waitcnt lgkmcnt(8)\n"
"s_add_u32 s81, s57, 47104\n"
"s_add_u32 s80, s57, 32768\n"
"s_add_u32 m0, s95, 147456\n"
"s_nop 0\n"
"buffer_load_dwordx4 v244, s[68:71], s80 offen lds\n"
"buffer_load_dwordx4 v245, s[68:71], s80 offen offset:1024 lds\n"
"buffer_load_dwordx4 v244, s[68:71], s81 offen offset:2048 lds\n"
"buffer_load_dwordx4 v245, s[68:71], s81 offen offset:3072 lds\n"
"ds_read_b64_tr_b16 v[80:81], v255 offset:32768\n"
"ds_read_b64_tr_b16 v[82:83], v255 offset:34816\n"
"ds_read_b64_tr_b16 v[84:85], v252 offset:32768\n"
"ds_read_b64_tr_b16 v[86:87], v252 offset:34816\n"
"ds_read_b64_tr_b16 v[88:89], v255 offset:33280\n"
"ds_read_b64_tr_b16 v[90:91], v255 offset:35328\n"
"ds_read_b64_tr_b16 v[92:93], v252 offset:33280\n"
"ds_read_b64_tr_b16 v[94:95], v252 offset:35328\n"
"s_waitcnt lgkmcnt(8)\n"
"s_add_u32 s81, s57, 79872\n"
"s_add_u32 s80, s57, 65536\n"
"s_add_u32 m0, s95, 131072\n"
"s_nop 0\n"
"buffer_load_dwordx4 v244, s[68:71], s80 offen lds\n"
"buffer_load_dwordx4 v245, s[68:71], s80 offen offset:1024 lds\n"
"buffer_load_dwordx4 v244, s[68:71], s81 offen offset:2048 lds\n"
"buffer_load_dwordx4 v245, s[68:71], s81 offen offset:3072 lds\n"
"ds_read_b64_tr_b16 v[96:97], v255 offset:16384\n"
"ds_read_b64_tr_b16 v[98:99], v255 offset:18432\n"
"ds_read_b64_tr_b16 v[100:101], v252 offset:16384\n"
"ds_read_b64_tr_b16 v[102:103], v252 offset:18432\n"
"ds_read_b64_tr_b16 v[104:105], v255 offset:16896\n"
"ds_read_b64_tr_b16 v[106:107], v255 offset:18944\n"
"ds_read_b64_tr_b16 v[108:109], v252 offset:16896\n"
"ds_read_b64_tr_b16 v[110:111], v252 offset:18944\n"
"s_waitcnt lgkmcnt(8)\n"
"s_add_u32 s81, s57, 112640\n"
"s_add_u32 s80, s57, 98304\n"
"s_add_u32 m0, s95, 114688\n"
"s_nop 0\n"
"buffer_load_dwordx4 v244, s[68:71], s80 offen lds\n"
"buffer_load_dwordx4 v245, s[68:71], s80 offen offset:1024 lds\n"
"buffer_load_dwordx4 v244, s[68:71], s81 offen offset:2048 lds\n"
"buffer_load_dwordx4 v245, s[68:71], s81 offen offset:3072 lds\n"
"ds_read_b64_tr_b16 v[112:113], v255\n"
"ds_read_b64_tr_b16 v[114:115], v255 offset:2048\n"
"ds_read_b64_tr_b16 v[116:117], v252\n"
"ds_read_b64_tr_b16 v[118:119], v252 offset:2048\n"
"ds_read_b64_tr_b16 v[120:121], v255 offset:512\n"
"ds_read_b64_tr_b16 v[122:123], v255 offset:2560\n"
"ds_read_b64_tr_b16 v[124:125], v252 offset:512\n"
"ds_read_b64_tr_b16 v[126:127], v252 offset:2560\n"
"s_waitcnt lgkmcnt(8)\n"
"s_add_u32 s81, s58, 14336\n"
"s_add_u32 m0, s95, 98304\n"
"s_nop 0\n"
"buffer_load_dwordx4 v244, s[68:71], s58 offen lds\n"
"buffer_load_dwordx4 v245, s[68:71], s58 offen offset:1024 lds\n"
"buffer_load_dwordx4 v244, s[68:71], s81 offen offset:2048 lds\n"
"buffer_load_dwordx4 v245, s[68:71], s81 offen offset:3072 lds\n"
"s_waitcnt lgkmcnt(0)\n"
"s_add_u32 s81, s58, 47104\n"
"s_add_u32 s80, s58, 32768\n"
"s_add_u32 m0, s95, 81920\n"
"s_nop 0\n"
"buffer_load_dwordx4 v244, s[68:71], s80 offen lds\n"
"buffer_load_dwordx4 v245, s[68:71], s80 offen offset:1024 lds\n"
"buffer_load_dwordx4 v244, s[68:71], s81 offen offset:2048 lds\n"
"buffer_load_dwordx4 v245, s[68:71], s81 offen offset:3072 lds\n"
"s_waitcnt vmcnt(20)\n"
"s_mov_b64 exec, 1\n"
"buffer_store_dwordx2 v[250:251], off, s[72:75], s65 sc1\n"
"s_mov_b64 exec, -1\n"
"s_lshl_b32 m0, s53, 9\n"
"s_nop 0\n"
"ds_write_addtid_b32 v248\n"
"s_waitcnt vmcnt(17)\n"
"ds_read_b64_tr_b16 v[128:129], v246\n"
"ds_read_b64_tr_b16 v[130:131], v246 offset:2048\n"
"ds_read_b64_tr_b16 v[132:133], v247\n"
"ds_read_b64_tr_b16 v[134:135], v247 offset:2048\n"
"ds_read_b64_tr_b16 v[136:137], v246 offset:512\n"
"ds_read_b64_tr_b16 v[138:139], v246 offset:2560\n"
"ds_read_b64_tr_b16 v[140:141], v247 offset:512\n"
"ds_read_b64_tr_b16 v[142:143], v247 offset:2560\n"
"s_waitcnt lgkmcnt(0)\n"
"s_add_u32 s81, s58, 79872\n"
"s_add_u32 s80, s58, 65536\n"
"s_add_u32 m0, s95, 147456\n"
"s_nop 0\n"
"buffer_load_dwordx4 v244, s[68:71], s80 offen lds\n"
"buffer_load_dwordx4 v245, s[68:71], s80 offen offset:1024 lds\n"
"buffer_load_dwordx4 v244, s[68:71], s81 offen offset:2048 lds\n"
"buffer_load_dwordx4 v245, s[68:71], s81 offen offset:3072 lds\n"
"s_waitcnt vmcnt(17)\n"
"ds_read_b64_tr_b16 v[144:145], v255 offset:49152\n"
"ds_read_b64_tr_b16 v[146:147], v255 offset:51200\n"
"ds_read_b64_tr_b16 v[148:149], v252 offset:49152\n"
"ds_read_b64_tr_b16 v[150:151], v252 offset:51200\n"
"ds_read_b64_tr_b16 v[152:153], v255 offset:49664\n"
"ds_read_b64_tr_b16 v[154:155], v255 offset:51712\n"
"ds_read_b64_tr_b16 v[156:157], v252 offset:49664\n"
"ds_read_b64_tr_b16 v[158:159], v252 offset:51712\n"
"s_waitcnt lgkmcnt(0)\n"
"s_add_u32 s81, s58, 112640\n"
"s_add_u32 s80, s58, 98304\n"
"s_add_u32 m0, s95, 131072\n"
"s_nop 0\n"
"buffer_load_dwordx4 v244, s[68:71], s80 offen lds\n"
"buffer_load_dwordx4 v245, s[68:71], s80 offen offset:1024 lds\n"
"buffer_load_dwordx4 v244, s[68:71], s81 offen offset:2048 lds\n"
"buffer_load_dwordx4 v245, s[68:71], s81 offen offset:3072 lds\n"
"s_waitcnt vmcnt(17)\n"
"ds_read_b64_tr_b16 v[160:161], v255 offset:32768\n"
"ds_read_b64_tr_b16 v[162:163], v255 offset:34816\n"
"ds_read_b64_tr_b16 v[164:165], v252 offset:32768\n"
"ds_read_b64_tr_b16 v[166:167], v252 offset:34816\n"
"ds_read_b64_tr_b16 v[168:169], v255 offset:33280\n"
"ds_read_b64_tr_b16 v[170:171], v255 offset:35328\n"
"ds_read_b64_tr_b16 v[172:173], v252 offset:33280\n"
"ds_read_b64_tr_b16 v[174:175], v252 offset:35328\n"
"s_waitcnt lgkmcnt(0)\n"
"s_add_u32 s81, s59, 14336\n"
"s_add_u32 m0, s95, 114688\n"
"s_nop 0\n"
"buffer_load_dwordx4 v244, s[68:71], s59 offen lds\n"
"buffer_load_dwordx4 v245, s[68:71], s59 offen offset:1024 lds\n"
"buffer_load_dwordx4 v244, s[68:71], s81 offen offset:2048 lds\n"
"buffer_load_dwordx4 v245, s[68:71], s81 offen offset:3072 lds\n"
"s_waitcnt vmcnt(17)\n"
"ds_read_b64_tr_b16 v[176:177], v255 offset:16384\n"
"ds_read_b64_tr_b16 v[178:179], v255 offset:18432\n"
"ds_read_b64_tr_b16 v[180:181], v252 offset:16384\n"
"ds_read_b64_tr_b16 v[182:183], v252 offset:18432\n"
"ds_read_b64_tr_b16 v[184:185], v255 offset:16896\n"
"ds_read_b64_tr_b16 v[186:187], v255 offset:18944\n"
"ds_read_b64_tr_b16 v[188:189], v252 offset:16896\n"
"ds_read_b64_tr_b16 v[190:191], v252 offset:18944\n"
"s_waitcnt lgkmcnt(0)\n"
"s_add_u32 s81, s59, 47104\n"
"s_add_u32 s80, s59, 32768\n"
"s_add_u32 m0, s95, 98304\n"
"s_nop 0\n"
"buffer_load_dwordx4 v244, s[68:71], s80 offen lds\n"
"buffer_load_dwordx4 v245, s[68:71], s80 offen offset:1024 lds\n"
"buffer_load_dwordx4 v244, s[68:71], s81 offen offset:2048 lds\n"
"buffer_load_dwordx4 v245, s[68:71], s81 offen offset:3072 lds\n"
"s_waitcnt vmcnt(17)\n"
"ds_read_b64_tr_b16 v[192:193], v255\n"
"ds_read_b64_tr_b16 v[194:195], v255 offset:2048\n"
"ds_read_b64_tr_b16 v[196:197], v252\n"
"ds_read_b64_tr_b16 v[198:199], v252 offset:2048\n"
"ds_read_b64_tr_b16 v[200:201], v255 offset:512\n"
"ds_read_b64_tr_b16 v[202:203], v255 offset:2560\n"
"ds_read_b64_tr_b16 v[204:205], v252 offset:512\n"
"ds_read_b64_tr_b16 v[206:207], v252 offset:2560\n"
"s_waitcnt lgkmcnt(0)\n"
"s_add_u32 s81, s59, 79872\n"
"s_add_u32 s80, s59, 65536\n"
"s_add_u32 m0, s95, 81920\n"
"s_nop 0\n"
"buffer_load_dwordx4 v244, s[68:71], s80 offen lds\n"
"buffer_load_dwordx4 v245, s[68:71], s80 offen offset:1024 lds\n"
"buffer_load_dwordx4 v244, s[68:71], s81 offen offset:2048 lds\n"
"buffer_load_dwordx4 v245, s[68:71], s81 offen offset:3072 lds\n"
"s_waitcnt vmcnt(16)\n"
"ds_read_b64_tr_b16 v[208:209], v246\n"
"ds_read_b64_tr_b16 v[210:211], v246 offset:2048\n"
"ds_read_b64_tr_b16 v[212:213], v247\n"
"ds_read_b64_tr_b16 v[214:215], v247 offset:2048\n"
"ds_read_b64_tr_b16 v[216:217], v246 offset:512\n"
"ds_read_b64_tr_b16 v[218:219], v246 offset:2560\n"
"ds_read_b64_tr_b16 v[220:221], v247 offset:512\n"
"ds_read_b64_tr_b16 v[222:223], v247 offset:2560\n"
"s_waitcnt lgkmcnt(0)\n"
"s_add_u32 s81, s59, 112640\n"
"s_add_u32 s80, s59, 98304\n"
"s_add_u32 m0, s95, 147456\n"
"s_nop 0\n"
"buffer_load_dwordx4 v244, s[68:71], s80 offen lds\n"
"buffer_load_dwordx4 v245, s[68:71], s80 offen offset:1024 lds\n"
"buffer_load_dwordx4 v244, s[68:71], s81 offen offset:2048 lds\n"
"buffer_load_dwordx4 v245, s[68:71], s81 offen offset:3072 lds\n"
"L_tailc:\n"
"s_setprio 0\n"
"L_cp0:\n"
"buffer_load_dwordx2 v[248:249], v253, s[72:75], 0 offen sc1\n"
"s_waitcnt vmcnt(0)\n"
"v_cmp_eq_u64 vcc, v[250:251], v[248:249]\n"
"s_mov_b32 s94, vcc_lo\n"
"s_bitcmp1_b32 s94, 0\n"
"s_cbranch_scc1 L_cd0\n"
"s_sleep 2\n"
"s_sub_u32 s93, s93, 1\n"
"s_cbranch_scc0 L_cp0\n"
"s_mov_b32 s93, 0\n"
"L_cd0:\n"
"buffer_load_dwordx4 v[224:227], v254, s[72:75], s60 offen sc1\n"
"buffer_load_dwordx4 v[228:231], v254, s[72:75], s60 offen offset:1024 sc1\n"
"buffer_load_dwordx4 v[232:235], v254, s[72:75], s60 offen offset:2048 sc1\n"
"s_add_u32 s60, s60, 3072\n"
"buffer_load_dwordx4 v[236:239], v254, s[72:75], s60 offen sc1\n"
"buffer_load_dwordx4 v[240:243], v254, s[72:75], s60 offen offset:1024 sc1\n"
"buffer_load_dwordx4 v[244:247], v254, s[72:75], s60 offen offset:2048 sc1\n"
"s_waitcnt vmcnt(5)\n"
"v_mfma_f32_16x16x32_bf16 v[0:3], v[224:227], v[48:51], 0\n"
"v_mfma_f32_16x16x32_bf16 v[12:15], v[224:227], v[52:55], 0\n"
"v_mfma_f32_16x16x32_bf16 v[24:27], v[224:227], v[56:59], 0\n"
"v_mfma_f32_16x16x32_bf16 v[36:39], v[224:227], v[60:63], 0\n"
"s_bitcmp1_b32 s94, 1\n"
"s_cbranch_scc1 L_cr1\n"
"buffer_load_dwordx2 v[248:249], v253, s[72:75], 0 offen sc1\n"
"L_cr1:\n"
"s_add_u32 s60, s60, 3072\n"
"buffer_load_dwordx4 v[224:227], v254, s[72:75], s60 offen sc1\n"
"s_waitcnt vmcnt(5)\n"
"v_mfma_f32_16x16x32_bf16 v[4:7], v[228:231], v[48:51], 0\n"
"v_mfma_f32_16x16x32_bf16 v[16:19], v[228:231], v[52:55], 0\n"
"v_mfma_f32_16x16x32_bf16 v[28:31], v[228:231], v[56:59], 0\n"
"v_mfma_f32_16x16x32_bf16 v[40:43], v[228:231], v[60:63], 0\n"
"buffer_load_dwordx4 v[228:231], v254, s[72:75], s60 offen offset:1024 sc1\n"
"s_waitcnt vmcnt(5)\n"
"v_mfma_f32_16x16x32_bf16 v[8:11], v[232:235], v[48:51], 0\n"
"v_mfma_f32_16x16x32_bf16 v[20:23], v[232:235], v[52:55], 0\n"
"v_mfma_f32_16x16x32_bf16 v[32:35], v[232:235], v[56:59], 0\n"
"v_mfma_f32_16x16x32_bf16 v[44:47], v[232:235], v[60:63], 0\n"
"buffer_load_dwordx4 v[232:235], v254, s[72:75], s60 offen offset:2048 sc1\n"
"ds_read_b64_tr_b16 v[48:49], v255 offset:49152\n"
"ds_read_b64_tr_b16 v[50:51], v255 offset:51200\n"
"ds_read_b64_tr_b16 v[52:53], v252 offset:49152\n"
"ds_read_b64_tr_b16 v[54:55], v252 offset:51200\n"
"ds_read_b64_tr_b16 v[56:57], v255 offset:49664\n"
"ds_read_b64_tr_b16 v[58:59], v255 offset:51712\n"
"ds_read_b64_tr_b16 v[60:61], v252 offset:49664\n"
"ds_read_b64_tr_b16 v[62:63], v252 offset:51712\n"
"s_waitcnt vmcnt(5)\n"
"v_mfma_f32_16x16x32_bf16 v[0:3], v[236:239], v[64:67], v[0:3]\n"
"v_mfma_f32_16x16x32_bf16 v[12:15], v[236:239], v[68:71], v[12:15]\n"
"v_mfma_f32_16x16x32_bf16 v[24:27], v[236:239], v[72:75], v[24:27]\n"
"v_mfma_f32_16x16x32_bf16 v[36:39], v[236:239], v[76:79], v[36:39]\n"
"s_add_u32 s60, s60, 3072\n"
"buffer_load_dwordx4 v[236:239], v254, s[72:75], s60 offen sc1\n"
"s_waitcnt vmcnt(5)\n"
"v_mfma_f32_16x16x32_bf16 v[4:7], v[240:243], v[64:67], v[4:7]\n"
"v_mfma_f32_16x16x32_bf16 v[16:19], v[240:243], v[68:71], v[16:19]\n"
"v_mfma_f32_16x16x32_bf16 v[28:31], v[240:243], v[72:75], v[28:31]\n"
"v_mfma_f32_16x16x32_bf16 v[40:43], v[240:243], v[76:79], v[40:43]\n"
"buffer_load_dwordx4 v[240:243], v254, s[72:75], s60 offen offset:1024 sc1\n"
"s_waitcnt vmcnt(5)\n"
"v_mfma_f32_16x16x32_bf16 v[8:11], v[244:247], v[64:67], v[8:11]\n"
"v_mfma_f32_16x16x32_bf16 v[20:23], v[244:247], v[68:71], v[20:23]\n"
"v_mfma_f32_16x16x32_bf16 v[32:35], v[244:247], v[72:75], v[32:35]\n"
"v_mfma_f32_16x16x32_bf16 v[44:47], v[244:247], v[76:79], v[44:47]\n"
"buffer_load_dwordx4 v[244:247], v254, s[72:75], s60 offen offset:2048 sc1\n"
"ds_read_b64_tr_b16 v[64:65], v255 offset:32768\n"
"ds_read_b64_tr_b16 v[66:67], v255 offset:34816\n"
"ds_read_b64_tr_b16 v[68:69], v252 offset:32768\n"
"ds_read_b64_tr_b16 v[70:71], v252 offset:34816\n"
"ds_read_b64_tr_b16 v[72:73], v255 offset:33280\n"
"ds_read_b64_tr_b16 v[74:75], v255 offset:35328\n"
"ds_read_b64_tr_b16 v[76:77], v252 offset:33280\n"
"ds_read_b64_tr_b16 v[78:79], v252 offset:35328\n"
"s_waitcnt vmcnt(5)\n"
"v_mfma_f32_16x16x32_bf16 v[0:3], v[224:227], v[80:83], v[0:3]\n"
"v_mfma_f32_16x16x32_bf16 v[12:15], v[224:227], v[84:87], v[12:15]\n"
"v_mfma_f32_16x16x32_bf16 v[24:27], v[224:227], v[88:91], v[24:27]\n"
"v_mfma_f32_16x16x32_bf16 v[36:39], v[224:227], v[92:95], v[36:39]\n"
"v_cmp_eq_u64 vcc, v[250:251], v[248:249]\n"
"s_mov_b32 s94, vcc_lo\n"
"s_bitcmp1_b32 s94, 1\n"
"s_cbranch_scc1 L_cd1\n"
"L_cp1:\n"
"buffer_load_dwordx2 v[248:249], v253, s[72:75], 0 offen sc1\n"
"s_waitcnt vmcnt(0)\n"
"v_cmp_eq_u64 vcc, v[250:251], v[248:249]\n"
"s_mov_b32 s94, vcc_lo\n"
"s_bitcmp1_b32 s94, 1\n"
"s_cbranch_scc1 L_cd1\n"
"s_sleep 2\n"
"s_sub_u32 s93, s93, 1\n"
"s_cbranch_scc0 L_cp1\n"
"s_mov_b32 s93, 0\n"
"L_cd1:\n"
"buffer_load_dwordx4 v[224:227], v254, s[72:75], s61 offen sc1\n"
"s_waitcnt vmcnt(5)\n"
"v_mfma_f32_16x16x32_bf16 v[4:7], v[228:231], v[80:83], v[4:7]\n"
"v_mfma_f32_16x16x32_bf16 v[16:19], v[228:231], v[84:87], v[16:19]\n"
"v_mfma_f32_16x16x32_bf16 v[28:31], v[228:231], v[88:91], v[28:31]\n"
"v_mfma_f32_16x16x32_bf16 v[40:43], v[228:231], v[92:95], v[40:43]\n"
"buffer_load_dwordx4 v[228:231], v254, s[72:75], s61 offen offset:1024 sc1\n"
"s_waitcnt vmcnt(5)\n"
"v_mfma_f32_16x16x32_bf16 v[8:11], v[232:235], v[80:83], v[8:11]\n"
"v_mfma_f32_16x16x32_bf16 v[20:23], v[232:235], v[84:87], v[20:23]\n"
"v_mfma_f32_16x16x32_bf16 v[32:35], v[232:235], v[88:91], v[32:35]\n"
"v_mfma_f32_16x16x32_bf16 v[44:47], v[232:235], v[92:95], v[44:47]\n"
"buffer_load_dwordx4 v[232:235], v254, s[72:75], s61 offen offset:2048 sc1\n"
"ds_read_b64_tr_b16 v[80:81], v255 offset:16384\n"
"ds_read_b64_tr_b16 v[82:83], v255 offset:18432\n"
"ds_read_b64_tr_b16 v[84:85], v252 offset:16384\n"
"ds_read_b64_tr_b16 v[86:87], v252 offset:18432\n"
"ds_read_b64_tr_b16 v[88:89], v255 offset:16896\n"
"ds_read_b64_tr_b16 v[90:91], v255 offset:18944\n"
"ds_read_b64_tr_b16 v[92:93], v252 offset:16896\n"
"ds_read_b64_tr_b16 v[94:95], v252 offset:18944\n"
"s_waitcnt vmcnt(5)\n"
"v_mfma_f32_16x16x32_bf16 v[0:3], v[236:239], v[96:99], v[0:3]\n"
"v_mfma_f32_16x16x32_bf16 v[12:15], v[236:239], v[100:103], v[12:15]\n"
"v_mfma_f32_16x16x32_bf16 v[24:27], v[236:239], v[104:107], v[24:27]\n"
"v_mfma_f32_16x16x32_bf16 v[36:39], v[236:239], v[108:111], v[36:39]\n"
"s_add_u32 s61, s61, 3072\n"
"buffer_load_dwordx4 v[236:239], v254, s[72:75], s61 offen sc1\n"
"s_waitcnt vmcnt(5)\n"
"v_mfma_f32_16x16x32_bf16 v[4:7], v[240:243], v[96:99], v[4:7]\n"
"v_mfma_f32_16x16x32_bf16 v[16:19], v[240:243], v[100:103], v[16:19]\n"
"v_mfma_f32_16x16x32_bf16 v[28:31], v[240:243], v[104:107], v[28:31]\n"
"v_mfma_f32_16x16x32_bf16 v[40:43], v[240:243], v[108:111], v[40:43]\n"
"buffer_load_dwordx4 v[240:243], v254, s[72:75], s61 offen offset:1024 sc1\n"
"s_waitcnt vmcnt(5)\n"
"v_mfma_f32_16x16x32_bf16 v[8:11], v[244:247], v[96:99], v[8:11]\n"
"v_mfma_f32_16x16x32_bf16 v[20:23], v[244:247], v[100:103], v[20:23]\n"
"v_mfma_f32_16x16x32_bf16 v[32:35], v[244:247], v[104:107], v[32:35]\n"
"v_mfma_f32_16x16x32_bf16 v[44:47], v[244:247], v[108:111], v[44:47]\n"
"buffer_load_dwordx4 v[244:247], v254, s[72:75], s61 offen offset:2048 sc1\n"
"ds_read_b64_tr_b16 v[96:97], v255\n"
"ds_read_b64_tr_b16 v[98:99], v255 offset:2048\n"
"ds_read_b64_tr_b16 v[100:101], v252\n"
"ds_read_b64_tr_b16 v[102:103], v252 offset:2048\n"
"ds_read_b64_tr_b16 v[104:105], v255 offset:512\n"
"ds_read_b64_tr_b16 v[106:107], v255 offset:2560\n"
"ds_read_b64_tr_b16 v[108:109], v252 offset:512\n"
"ds_read_b64_tr_b16 v[110:111], v252 offset:2560\n"
"v_add_u32 v255, 0x10000, v255\n"
"v_add_u32 v252, 0x10000, v252\n"
"s_waitcnt vmcnt(5)\n"
"v_mfma_f32_16x16x32_bf16 v[0:3], v[224:227], v[112:115], v[0:3]\n"
"v_mfma_f32_16x16x32_bf16 v[12:15], v[224:227], v[116:119], v[12:15]\n"
"v_mfma_f32_16x16x32_bf16 v[24:27], v[224:227], v[120:123], v[24:27]\n"
"v_mfma_f32_16x16x32_bf16 v[36:39], v[224:227], v[124:127], v[36:39]\n"
"s_bitcmp1_b32 s94, 2\n"
"s_cbranch_scc1 L_cr2\n"
"buffer_load_dwordx2 v[248:249], v253, s[72:75], 0 offen sc1\n"
"L_cr2:\n"
"s_add_u32 s61, s61, 3072\n"
"buffer_load_dwordx4 v[224:227], v254, s[72:75], s61 offen sc1\n"
"s_waitcnt vmcnt(5)\n"
"v_mfma_f32_16x16x32_bf16 v[4:7], v[228:231], v[112:115], v[4:7]\n"
"v_mfma_f32_16x16x32_bf16 v[16:19], v[228:231], v[116:119], v[16:19]\n"
"v_mfma_f32_16x16x32_bf16 v[28:31], v[228:231], v[120:123], v[28:31]\n"
"v_mfma_f32_16x16x32_bf16 v[40:43], v[228:231], v[124:127], v[40:43]\n"
"buffer_load_dwordx4 v[228:231], v254, s[72:75], s61 offen offset:1024 sc1\n"
"s_waitcnt vmcnt(5)\n"
"v_mfma_f32_16x16x32_bf16 v[8:11], v[232:235], v[112:115], v[8:11]\n"
"v_mfma_f32_16x16x32_bf16 v[20:23], v[232:235], v[116:119], v[20:23]\n"
"v_mfma_f32_16x16x32_bf16 v[32:35], v[232:235], v[120:123], v[32:35]\n"
"v_mfma_f32_16x16x32_bf16 v[44:47], v[232:235], v[124:127], v[44:47]\n"
"buffer_load_dwordx4 v[232:235], v254, s[72:75], s61 offen offset:2048 sc1\n"
"ds_read_b64_tr_b16 v[112:113], v255\n"
"ds_read_b64_tr_b16 v[114:115], v255 offset:2048\n"
"ds_read_b64_tr_b16 v[116:117], v252\n"
"ds_read_b64_tr_b16 v[118:119], v252 offset:2048\n"
"ds_read_b64_tr_b16 v[120:121], v255 offset:512\n"
"ds_read_b64_tr_b16 v[122:123], v255 offset:2560\n"
"ds_read_b64_tr_b16 v[124:125], v252 offset:512\n"
"ds_read_b64_tr_b16 v[126:127], v252 offset:2560\n"
"s_waitcnt vmcnt(5)\n"
"v_mfma_f32_16x16x32_bf16 v[0:3], v[236:239], v[128:131], v[0:3]\n"
"v_mfma_f32_16x16x32_bf16 v[12:15], v[236:239], v[132:135], v[12:15]\n"
"v_mfma_f32_16x16x32_bf16 v[24:27], v[236:239], v[136:139], v[24:27]\n"
"v_mfma_f32_16x16x32_bf16 v[36:39], v[236:239], v[140:143], v[36:39]\n"
"s_add_u32 s61, s61, 3072\n"
"buffer_load_dwordx4 v[236:239], v254, s[72:75], s61 offen sc1\n"
"s_waitcnt vmcnt(5)\n"
"v_mfma_f32_16x16x32_bf16 v[4:7], v[240:243], v[128:131], v[4:7]\n"
"v_mfma_f32_16x16x32_bf16 v[16:19], v[240:243], v[132:135], v[16:19]\n"
"v_mfma_f32_16x16x32_bf16 v[28:31], v[240:243], v[136:139], v[28:31]\n"
"v_mfma_f32_16x16x32_bf16 v[40:43], v[240:243], v[140:143], v[40:43]\n"
"buffer_load_dwordx4 v[240:243], v254, s[72:75], s61 offen offset:1024 sc1\n"
"s_waitcnt vmcnt(5)\n"
"v_mfma_f32_16x16x32_bf16 v[8:11], v[244:247], v[128:131], v[8:11]\n"
"v_mfma_f32_16x16x32_bf16 v[20:23], v[244:247], v[132:135], v[20:23]\n"
"v_mfma_f32_16x16x32_bf16 v[32:35], v[244:247], v[136:139], v[32:35]\n"
"v_mfma_f32_16x16x32_bf16 v[44:47], v[244:247], v[140:143], v[44:47]\n"
"buffer_load_dwordx4 v[244:247], v254, s[72:75], s61 offen offset:2048 sc1\n"
"s_waitcnt vmcnt(5)\n"
"v_mfma_f32_16x16x32_bf16 v[0:3], v[224:227], v[144:147], v[0:3]\n"
"v_mfma_f32_16x16x32_bf16 v[12:15], v[224:227], v[148:151], v[12:15]\n"
"v_mfma_f32_16x16x32_bf16 v[24:27], v[224:227], v[152:155], v[24:27]\n"
"v_mfma_f32_16x16x32_bf16 v[36:39], v[224:227], v[156:159], v[36:39]\n"
"v_cmp_eq_u64 vcc, v[250:251], v[248:249]\n"
"s_mov_b32 s94, vcc_lo\n"
"s_bitcmp1_b32 s94, 2\n"
"s_cbranch_scc1 L_cd2\n"
"L_cp2:\n"
"buffer_load_dwordx2 v[248:249], v253, s[72:75], 0 offen sc1\n"
"s_waitcnt vmcnt(0)\n"
"v_cmp_eq_u64 vcc, v[250:251], v[248:249]\n"
"s_mov_b32 s94, vcc_lo\n"
"s_bitcmp1_b32 s94, 2\n"
"s_cbranch_scc1 L_cd2\n"
"s_sleep 2\n"
"s_sub_u32 s93, s93, 1\n"
"s_cbranch_scc0 L_cp2\n"
"s_mov_b32 s93, 0\n"
"L_cd2:\n"
"buffer_load_dwordx4 v[224:227], v254, s[72:75], s62 offen sc1\n"
"s_waitcnt vmcnt(5)\n"
"v_mfma_f32_16x16x32_bf16 v[4:7], v[228:231], v[144:147], v[4:7]\n"
"v_mfma_f32_16x16x32_bf16 v[16:19], v[228:231], v[148:151], v[16:19]\n"
"v_mfma_f32_16x16x32_bf16 v[28:31], v[228:231], v[152:155], v[28:31]\n"
"v_mfma_f32_16x16x32_bf16 v[40:43], v[228:231], v[156:159], v[40:43]\n"
"buffer_load_dwordx4 v[228:231], v254, s[72:75], s62 offen offset:1024 sc1\n"
"s_waitcnt vmcnt(5)\n"
"v_mfma_f32_16x16x32_bf16 v[8:11], v[232:235], v[144:147], v[8:11]\n"
"v_mfma_f32_16x16x32_bf16 v[20:23], v[232:235], v[148:151], v[20:23]\n"
"v_mfma_f32_16x16x32_bf16 v[32:35], v[232:235], v[152:155], v[32:35]\n"
"v_mfma_f32_16x16x32_bf16 v[44:47], v[232:235], v[156:159], v[44:47]\n"
"buffer_load_dwordx4 v[232:235], v254, s[72:75], s62 offen offset:2048 sc1\n"
"s_waitcnt vmcnt(5)\n"
"v_mfma_f32_16x16x32_bf16 v[0:3], v[236:239], v[160:163], v[0:3]\n"
"v_mfma_f32_16x16x32_bf16 v[12:15], v[236:239], v[164:167], v[12:15]\n"
"v_mfma_f32_16x16x32_bf16 v[24:27], v[236:239], v[168:171], v[24:27]\n"
"v_mfma_f32_16x16x32_bf16 v[36:39], v[236:239], v[172:175], v[36:39]\n"
"s_add_u32 s62, s62, 3072\n"
"buffer_load_dwordx4 v[236:239], v254, s[72:75], s62 offen sc1\n"
"s_waitcnt vmcnt(5)\n"
"v_mfma_f32_16x16x32_bf16 v[4:7], v[240:243], v[160:163], v[4:7]\n"
"v_mfma_f32_16x16x32_bf16 v[16:19], v[240:243], v[164:167], v[16:19]\n"
"v_mfma_f32_16x16x32_bf16 v[28:31], v[240:243], v[168:171], v[28:31]\n"
"v_mfma_f32_16x16x32_bf16 v[40:43], v[240:243], v[172:175], v[40:43]\n"
"buffer_load_dwordx4 v[240:243], v254, s[72:75], s62 offen offset:1024 sc1\n"
"s_waitcnt vmcnt(5)\n"
"v_mfma_f32_16x16x32_bf16 v[8:11], v[244:247], v[160:163], v[8:11]\n"
"v_mfma_f32_16x16x32_bf16 v[20:23], v[244:247], v[164:167], v[20:23]\n"
"v_mfma_f32_16x16x32_bf16 v[32:35], v[244:247], v[168:171], v[32:35]\n"
"v_mfma_f32_16x16x32_bf16 v[44:47], v[244:247], v[172:175], v[44:47]\n"
"buffer_load_dwordx4 v[244:247], v254, s[72:75], s62 offen offset:2048 sc1\n"
"s_waitcnt vmcnt(5)\n"
"v_mfma_f32_16x16x32_bf16 v[0:3], v[224:227], v[176:179], v[0:3]\n"
"v_mfma_f32_16x16x32_bf16 v[12:15], v[224:227], v[180:183], v[12:15]\n"
"v_mfma_f32_16x16x32_bf16 v[24:27], v[224:227], v[184:187], v[24:27]\n"
"v_mfma_f32_16x16x32_bf16 v[36:39], v[224:227], v[188:191], v[36:39]\n"
"s_bitcmp1_b32 s94, 3\n"
"s_cbranch_scc1 L_cr3\n"
"buffer_load_dwordx2 v[248:249], v253, s[72:75], 0 offen sc1\n"
"L_cr3:\n"
"s_add_u32 s62, s62, 3072\n"
"buffer_load_dwordx4 v[224:227], v254, s[72:75], s62 offen sc1\n"
"s_waitcnt vmcnt(5)\n"
"v_mfma_f32_16x16x32_bf16 v[4:7], v[228:231], v[176:179], v[4:7]\n"
"v_mfma_f32_16x16x32_bf16 v[16:19], v[228:231], v[180:183], v[16:19]\n"
"v_mfma_f32_16x16x32_bf16 v[28:31], v[228:231], v[184:187], v[28:31]\n"
"v_mfma_f32_16x16x32_bf16 v[40:43], v[228:231], v[188:191], v[40:43]\n"
"buffer_load_dwordx4 v[228:231], v254, s[72:75], s62 offen offset:1024 sc1\n"
"s_waitcnt vmcnt(5)\n"
"v_mfma_f32_16x16x32_bf16 v[8:11], v[232:235], v[176:179], v[8:11]\n"
"v_mfma_f32_16x16x32_bf16 v[20:23], v[232:235], v[180:183], v[20:23]\n"
"v_mfma_f32_16x16x32_bf16 v[32:35], v[232:235], v[184:187], v[32:35]\n"
"v_mfma_f32_16x16x32_bf16 v[44:47], v[232:235], v[188:191], v[44:47]\n"
"buffer_load_dwordx4 v[232:235], v254, s[72:75], s62 offen offset:2048 sc1\n"
"s_waitcnt vmcnt(5)\n"
"v_mfma_f32_16x16x32_bf16 v[0:3], v[236:239], v[192:195], v[0:3]\n"
"v_mfma_f32_16x16x32_bf16 v[12:15], v[236:239], v[196:199], v[12:15]\n"
"v_mfma_f32_16x16x32_bf16 v[24:27], v[236:239], v[200:203], v[24:27]\n"
"v_mfma_f32_16x16x32_bf16 v[36:39], v[236:239], v[204:207], v[36:39]\n"
"s_add_u32 s62, s62, 3072\n"
"buffer_load_dwordx4 v[236:239], v254, s[72:75], s62 offen sc1\n"
"s_waitcnt vmcnt(5)\n"
"v_mfma_f32_16x16x32_bf16 v[4:7], v[240:243], v[192:195], v[4:7]\n"
"v_mfma_f32_16x16x32_bf16 v[16:19], v[240:243], v[196:199], v[16:19]\n"
"v_mfma_f32_16x16x32_bf16 v[28:31], v[240:243], v[200:203], v[28:31]\n"
"v_mfma_f32_16x16x32_bf16 v[40:43], v[240:243], v[204:207], v[40:43]\n"
"buffer_load_dwordx4 v[240:243], v254, s[72:75], s62 offen offset:1024 sc1\n"
"s_waitcnt vmcnt(5)\n"
"v_mfma_f32_16x16x32_bf16 v[8:11], v[244:247], v[192:195], v[8:11]\n"
"v_mfma_f32_16x16x32_bf16 v[20:23], v[244:247], v[196:199], v[20:23]\n"
"v_mfma_f32_16x16x32_bf16 v[32:35], v[244:247], v[200:203], v[32:35]\n"
"v_mfma_f32_16x16x32_bf16 v[44:47], v[244:247], v[204:207], v[44:47]\n"
"buffer_load_dwordx4 v[244:247], v254, s[72:75], s62 offen offset:2048 sc1\n"
"s_waitcnt vmcnt(5)\n"
"v_mfma_f32_16x16x32_bf16 v[0:3], v[224:227], v[208:211], v[0:3]\n"
"v_mfma_f32_16x16x32_bf16 v[12:15], v[224:227], v[212:215], v[12:15]\n"
"v_mfma_f32_16x16x32_bf16 v[24:27], v[224:227], v[216:219], v[24:27]\n"
"v_mfma_f32_16x16x32_bf16 v[36:39], v[224:227], v[220:223], v[36:39]\n"
"v_cmp_eq_u64 vcc, v[250:251], v[248:249]\n"
"s_mov_b32 s94, vcc_lo\n"
"s_bitcmp1_b32 s94, 3\n"
"s_cbranch_scc1 L_cd3\n"
"L_cp3:\n"
"buffer_load_dwordx2 v[248:249], v253, s[72:75], 0 offen sc1\n"
"s_waitcnt vmcnt(0)\n"
"v_cmp_eq_u64 vcc, v[250:251], v[248:249]\n"
"s_mov_b32 s94, vcc_lo\n"
"s_bitcmp1_b32 s94, 3\n"
"s_cbranch_scc1 L_cd3\n"
"s_sleep 2\n"
"s_sub_u32 s93, s93, 1\n"
"s_cbranch_scc0 L_cp3\n"
"s_mov_b32 s93, 0\n"
"L_cd3:\n"
"buffer_load_dwordx4 v[224:227], v254, s[72:75], s63 offen sc1\n"
"s_waitcnt vmcnt(5)\n"
"v_mfma_f32_16x16x32_bf16 v[4:7], v[228:231], v[208:211], v[4:7]\n"
"v_mfma_f32_16x16x32_bf16 v[16:19], v[228:231], v[212:215], v[16:19]\n"
"v_mfma_f32_16x16x32_bf16 v[28:31], v[228:231], v[216:219], v[28:31]\n"
"v_mfma_f32_16x16x32_bf16 v[40:43], v[228:231], v[220:223], v[40:43]\n"
"buffer_load_dwordx4 v[228:231], v254, s[72:75], s63 offen offset:1024 sc1\n"
"s_waitcnt vmcnt(5)\n"
"v_mfma_f32_16x16x32_bf16 v[8:11], v[232:235], v[208:211], v[8:11]\n"
"v_mfma_f32_16x16x32_bf16 v[20:23], v[232:235], v[212:215], v[20:23]\n"
"v_mfma_f32_16x16x32_bf16 v[32:35], v[232:235], v[216:219], v[32:35]\n"
"v_mfma_f32_16x16x32_bf16 v[44:47], v[232:235], v[220:223], v[44:47]\n"
"buffer_load_dwordx4 v[232:235], v254, s[72:75], s63 offen offset:2048 sc1\n"
"s_waitcnt lgkmcnt(14)\n"
"s_waitcnt vmcnt(5)\n"
"v_mfma_f32_16x16x32_bf16 v[0:3], v[236:239], v[48:51], v[0:3]\n"
"v_mfma_f32_16x16x32_bf16 v[12:15], v[236:239], v[52:55], v[12:15]\n"
"v_mfma_f32_16x16x32_bf16 v[24:27], v[236:239], v[56:59], v[24:27]\n"
"v_mfma_f32_16x16x32_bf16 v[36:39], v[236:239], v[60:63], v[36:39]\n"
"s_add_u32 s63, s63, 3072\n"
"buffer_load_dwordx4 v[236:239], v254, s[72:75], s63 offen sc1\n"
"s_waitcnt vmcnt(5)\n"
"v_mfma_f32_16x16x32_bf16 v[4:7], v[240:243], v[48:51], v[4:7]\n"
"v_mfma_f32_16x16x32_bf16 v[16:19], v[240:243], v[52:55], v[16:19]\n"
"v_mfma_f32_16x16x32_bf16 v[28:31], v[240:243], v[56:59], v[28:31]\n"
"v_mfma_f32_16x16x32_bf16 v[40:43], v[240:243], v[60:63], v[40:43]\n"
"buffer_load_dwordx4 v[240:243], v254, s[72:75], s63 offen offset:1024 sc1\n"
"s_waitcnt vmcnt(5)\n"
"v_mfma_f32_16x16x32_bf16 v[8:11], v[244:247], v[48:51], v[8:11]\n"
"v_mfma_f32_16x16x32_bf16 v[20:23], v[244:247], v[52:55], v[20:23]\n"
"v_mfma_f32_16x16x32_bf16 v[32:35], v[244:247], v[56:59], v[32:35]\n"
"v_mfma_f32_16x16x32_bf16 v[44:47], v[244:247], v[60:63], v[44:47]\n"
"buffer_load_dwordx4 v[244:247], v254, s[72:75], s63 offen offset:2048 sc1\n"
"s_waitcnt vmcnt(5)\n"
"v_mfma_f32_16x16x32_bf16 v[0:3], v[224:227], v[64:67], v[0:3]\n"
"v_mfma_f32_16x16x32_bf16 v[12:15], v[224:227], v[68:71], v[12:15]\n"
"v_mfma_f32_16x16x32_bf16 v[24:27], v[224:227], v[72:75], v[24:27]\n"
"v_mfma_f32_16x16x32_bf16 v[36:39], v[224:227], v[76:79], v[36:39]\n"
"s_add_u32 s63, s63, 3072\n"
"buffer_load_dwordx4 v[224:227], v254, s[72:75], s63 offen sc1\n"
"s_waitcnt vmcnt(5)\n"
"v_mfma_f32_16x16x32_bf16 v[4:7], v[228:231], v[64:67], v[4:7]\n"
"v_mfma_f32_16x16x32_bf16 v[16:19], v[228:231], v[68:71], v[16:19]\n"
"v_mfma_f32_16x16x32_bf16 v[28:31], v[228:231], v[72:75], v[28:31]\n"
"v_mfma_f32_16x16x32_bf16 v[40:43], v[228:231], v[76:79], v[40:43]\n"
"buffer_load_dwordx4 v[228:231], v254, s[72:75], s63 offen offset:1024 sc1\n"
"s_waitcnt vmcnt(5)\n"
"v_mfma_f32_16x16x32_bf16 v[8:11], v[232:235], v[64:67], v[8:11]\n"
"v_mfma_f32_16x16x32_bf16 v[20:23], v[232:235], v[68:71], v[20:23]\n"
"v_mfma_f32_16x16x32_bf16 v[32:35], v[232:235], v[72:75], v[32:35]\n"
"v_mfma_f32_16x16x32_bf16 v[44:47], v[232:235], v[76:79], v[44:47]\n"
"buffer_load_dwordx4 v[232:235], v254, s[72:75], s63 offen offset:2048 sc1\n"
"s_lshr_b32 s80, s51, 1\n"
"s_and_b32 s81, s51, 1\n"
"s_lshl_b32 s81, s81, 11\n"
"s_lshl_b32 s82, s80, 14\n"
"s_add_u32 s82, s82, s81\n"
"v_add_u32 v196, s82, v254\n"
"v_add_u32 v197, 0x10000, v196\n"
"v_bfe_u32 v198, v254, 4, 2\n"
"v_lshrrev_b32 v194, 8, v254\n"
"v_lshl_or_b32 v198, v194, 2, v198\n"
"v_lshlrev_b32 v198, 3, v198\n"
"v_and_b32 v199, 0xc0, v254\n"
"v_lshl_or_b32 v198, v199, 6, v198\n"
"s_mov_b32 s96, s82\n"
"s_waitcnt vmcnt(5)\n"
"v_mfma_f32_16x16x32_bf16 v[0:3], v[236:239], v[80:83], v[0:3]\n"
"v_mfma_f32_16x16x32_bf16 v[12:15], v[236:239], v[84:87], v[12:15]\n"
"v_mfma_f32_16x16x32_bf16 v[24:27], v[236:239], v[88:91], v[24:27]\n"
"v_mfma_f32_16x16x32_bf16 v[36:39], v[236:239], v[92:95], v[36:39]\n"
"s_add_u32 s63, s63, 3072\n"
"buffer_load_dwordx4 v[236:239], v254, s[72:75], s63 offen sc1\n"
"s_waitcnt vmcnt(5)\n"
"v_mfma_f32_16x16x32_bf16 v[4:7], v[240:243], v[80:83], v[4:7]\n"
"v_mfma_f32_16x16x32_bf16 v[16:19], v[240:243], v[84:87], v[16:19]\n"
"v_mfma_f32_16x16x32_bf16 v[28:31], v[240:243], v[88:91], v[28:31]\n"
"v_mfma_f32_16x16x32_bf16 v[40:43], v[240:243], v[92:95], v[40:43]\n"
"buffer_load_dwordx4 v[240:243], v254, s[72:75], s63 offen offset:1024 sc1\n"
"s_waitcnt vmcnt(5)\n"
"v_mfma_f32_16x16x32_bf16 v[8:11], v[244:247], v[80:83], v[8:11]\n"
"v_mfma_f32_16x16x32_bf16 v[20:23], v[244:247], v[84:87], v[20:23]\n"
"v_mfma_f32_16x16x32_bf16 v[32:35], v[244:247], v[88:91], v[32:35]\n"
"v_mfma_f32_16x16x32_bf16 v[44:47], v[244:247], v[92:95], v[44:47]\n"
"buffer_load_dwordx4 v[244:247], v254, s[72:75], s63 offen offset:2048 sc1\n"
"s_waitcnt vmcnt(5)\n"
"v_mfma_f32_16x16x32_bf16 v[0:3], v[224:227], v[96:99], v[0:3]\n"
"s_waitcnt lgkmcnt(12)\n"
"v_mfma_f32_16x16x32_bf16 v[12:15], v[224:227], v[100:103], v[12:15]\n"
"s_waitcnt lgkmcnt(10)\n"
"v_mfma_f32_16x16x32_bf16 v[24:27], v[224:227], v[104:107], v[24:27]\n"
"s_waitcnt lgkmcnt(8)\n"
"v_mfma_f32_16x16x32_bf16 v[36:39], v[224:227], v[108:111], v[36:39]\n"
"s_waitcnt vmcnt(4)\n"
"v_mfma_f32_16x16x32_bf16 v[4:7], v[228:231], v[96:99], v[4:7]\n"
"v_mfma_f32_16x16x32_bf16 v[16:19], v[228:231], v[100:103], v[16:19]\n"
"v_mfma_f32_16x16x32_bf16 v[28:31], v[228:231], v[104:107], v[28:31]\n"
"v_mfma_f32_16x16x32_bf16 v[40:43], v[228:231], v[108:111], v[40:43]\n"
"s_waitcnt vmcnt(3)\n"
"v_mfma_f32_16x16x32_bf16 v[8:11], v[232:235], v[96:99], v[8:11]\n"
"v_mfma_f32_16x16x32_bf16 v[20:23], v[232:235], v[100:103], v[20:23]\n"
"v_mfma_f32_16x16x32_bf16 v[32:35], v[232:235], v[104:107], v[32:35]\n"
"v_mfma_f32_16x16x32_bf16 v[44:47], v[232:235], v[108:111], v[44:47]\n"
"s_waitcnt lgkmcnt(6)\n"
"s_waitcnt vmcnt(2)\n"
"v_mfma_f32_16x16x32_bf16 v[0:3], v[236:239], v[112:115], v[0:3]\n"
"s_waitcnt lgkmcnt(4)\n"
"v_mfma_f32_16x16x32_bf16 v[12:15], v[236:239], v[116:119], v[12:15]\n"
"s_waitcnt lgkmcnt(2)\n"
"v_mfma_f32_16x16x32_bf16 v[24:27], v[236:239], v[120:123], v[24:27]\n"
"s_waitcnt lgkmcnt(0)\n"
"v_mfma_f32_16x16x32_bf16 v[36:39], v[236:239], v[124:127], v[36:39]\n"
"s_waitcnt vmcnt(1)\n"
"v_mfma_f32_16x16x32_bf16 v[4:7], v[240:243], v[112:115], v[4:7]\n"
"v_mfma_f32_16x16x32_bf16 v[16:19], v[240:243], v[116:119], v[16:19]\n"
"v_mfma_f32_16x16x32_bf16 v[28:31], v[240:243], v[120:123], v[28:31]\n"
"v_mfma_f32_16x16x32_bf16 v[40:43], v[240:243], v[124:127], v[40:43]\n"
"s_waitcnt vmcnt(0)\n"
"v_mfma_f32_16x16x32_bf16 v[8:11], v[244:247], v[112:115], v[8:11]\n"
"v_mfma_f32_16x16x32_bf16 v[20:23], v[244:247], v[116:119], v[20:23]\n"
"v_mfma_f32_16x16x32_bf16 v[32:35], v[244:247], v[120:123], v[32:35]\n"
"v_mfma_f32_16x16x32_bf16 v[44:47], v[244:247], v[124:127], v[44:47]\n"
"s_add_u32 m0, s95, 53120\n"
"s_nop 0\n"
"ds_write_addtid_b32 v0 offset:28800\n"
"ds_write_addtid_b32 v1 offset:29824\n"
"ds_write_addtid_b32 v2 offset:30848\n"
"ds_write_addtid_b32 v3 offset:31872\n"
"ds_write_addtid_b32 v12 offset:29056\n"
"ds_write_addtid_b32 v13 offset:30080\n"
"ds_write_addtid_b32 v14 offset:31104\n"
"ds_write_addtid_b32 v15 offset:32128\n"
"ds_write_addtid_b32 v24 offset:29312\n"
"ds_write_addtid_b32 v25 offset:30336\n"
"ds_write_addtid_b32 v26 offset:31360\n"
"ds_write_addtid_b32 v27 offset:32384\n"
"ds_write_addtid_b32 v36 offset:29568\n"
"ds_write_addtid_b32 v37 offset:30592\n"
"ds_write_addtid_b32 v38 offset:31616\n"
"ds_write_addtid_b32 v39 offset:32640\n"
"ds_write_addtid_b32 v4 offset:45184\n"
"ds_write_addtid_b32 v5 offset:46208\n"
"ds_write_addtid_b32 v6 offset:47232\n"
"ds_write_addtid_b32 v7 offset:48256\n"
"ds_write_addtid_b32 v16 offset:45440\n"
"ds_write_addtid_b32 v17 offset:46464\n"
"ds_write_addtid_b32 v18 offset:47488\n"
"ds_write_addtid_b32 v19 offset:48512\n"
"ds_write_addtid_b32 v28 offset:45696\n"
"ds_write_addtid_b32 v29 offset:46720\n"
"ds_write_addtid_b32 v30 offset:47744\n"
"ds_write_addtid_b32 v31 offset:48768\n"
"ds_write_addtid_b32 v40 offset:45952\n"
"ds_write_addtid_b32 v41 offset:46976\n"
"ds_write_addtid_b32 v42 offset:48000\n"
"ds_write_addtid_b32 v43 offset:49024\n"
"ds_write_addtid_b32 v8 offset:61568\n"
"ds_write_addtid_b32 v9 offset:62592\n"
"ds_write_addtid_b32 v10 offset:63616\n"
"ds_write_addtid_b32 v11 offset:64640\n"
"ds_write_addtid_b32 v20 offset:61824\n"
"ds_write_addtid_b32 v21 offset:62848\n"
"ds_write_addtid_b32 v22 offset:63872\n"
"ds_write_addtid_b32 v23 offset:64896\n"
"ds_write_addtid_b32 v32 offset:62080\n"
"ds_write_addtid_b32 v33 offset:63104\n"
"ds_write_addtid_b32 v34 offset:64128\n"
"ds_write_addtid_b32 v35 offset:65152\n"
"ds_write_addtid_b32 v44 offset:62336\n"
"ds_write_addtid_b32 v45 offset:63360\n"
"ds_write_addtid_b32 v46 offset:64384\n"
"ds_write_addtid_b32 v47 offset:65408\n"
"s_waitcnt lgkmcnt(0)\n"
"L_red:\n"
"s_barrier\n"
"s_cmp_ge_u32 s51, 6\n"
"s_cbranch_scc1 L_end\n"
"ds_read_b128 v[48:51], v196 offset:32768\n"
"ds_read_b128 v[52:55], v196 offset:33792\n"
"ds_read_b128 v[56:59], v196 offset:36864\n"
"ds_read_b128 v[60:63], v196 offset:37888\n"
"ds_read_b128 v[64:67], v196 offset:40960\n"
"ds_read_b128 v[68:71], v196 offset:41984\n"
"ds_read_b128 v[72:75], v196 offset:45056\n"
"ds_read_b128 v[76:79], v196 offset:46080\n"
"ds_read_b128 v[80:83], v197 offset:16384\n"
"ds_read_b128 v[84:87], v197 offset:17408\n"
"ds_read_b128 v[88:91], v197 offset:20480\n"
"ds_read_b128 v[92:95], v197 offset:21504\n"
"ds_read_b128 v[96:99], v197 offset:24576\n"
"ds_read_b128 v[100:103], v197 offset:25600\n"
"ds_read_b128 v[104:107], v197 offset:28672\n"
"ds_read_b128 v[108:111], v197 offset:29696\n"
"s_waitcnt lgkmcnt(13)\n"
"v_pk_add_f32 v[48:49], v[48:49], v[56:57]\n"
"v_pk_add_f32 v[50:51], v[50:51], v[58:59]\n"
"s_waitcnt lgkmcnt(12)\n"
"v_pk_add_f32 v[52:53], v[52:53], v[60:61]\n"
"v_pk_add_f32 v[54:55], v[54:55], v[62:63]\n"
"s_waitcnt lgkmcnt(11)\n"
"v_pk_add_f32 v[48:49], v[48:49], v[64:65]\n"
"v_pk_add_f32 v[50:51], v[50:51], v[66:67]\n"
"s_waitcnt lgkmcnt(10)\n"
"v_pk_add_f32 v[52:53], v[52:53], v[68:69]\n"
"v_pk_add_f32 v[54:55], v[54:55], v[70:71]\n"
"s_waitcnt lgkmcnt(9)\n"
"v_pk_add_f32 v[48:49], v[48:49], v[72:73]\n"
"v_pk_add_f32 v[50:51], v[50:51], v[74:75]\n"
"s_waitcnt lgkmcnt(8)\n"
"v_pk_add_f32 v[52:53], v[52:53], v[76:77]\n"
"v_pk_add_f32 v[54:55], v[54:55], v[78:79]\n"
"s_waitcnt lgkmcnt(7)\n"
"v_pk_add_f32 v[48:49], v[48:49], v[80:81]\n"
"v_pk_add_f32 v[50:51], v[50:51], v[82:83]\n"
"s_waitcnt lgkmcnt(6)\n"
"v_pk_add_f32 v[52:53], v[52:53], v[84:85]\n"
"v_pk_add_f32 v[54:55], v[54:55], v[86:87]\n"
"s_waitcnt lgkmcnt(5)\n"
"v_pk_add_f32 v[48:49], v[48:49], v[88:89]\n"
"v_pk_add_f32 v[50:51], v[50:51], v[90:91]\n"
"s_waitcnt lgkmcnt(4)\n"
"v_pk_add_f32 v[52:53], v[52:53], v[92:93]\n"
"v_pk_add_f32 v[54:55], v[54:55], v[94:95]\n"
"s_waitcnt lgkmcnt(3)\n"
"v_pk_add_f32 v[48:49], v[48:49], v[96:97]\n"
"v_pk_add_f32 v[50:51], v[50:51], v[98:99]\n"
"s_waitcnt lgkmcnt(2)\n"
"v_pk_add_f32 v[52:53], v[52:53], v[100:101]\n"
"v_pk_add_f32 v[54:55], v[54:55], v[102:103]\n"
"s_waitcnt lgkmcnt(1)\n"
"v_pk_add_f32 v[48:49], v[48:49], v[104:105]\n"
"v_pk_add_f32 v[50:51], v[50:51], v[106:107]\n"
"s_waitcnt lgkmcnt(0)\n"
"v_pk_add_f32 v[52:53], v[52:53], v[108:109]\n"
"v_pk_add_f32 v[54:55], v[54:55], v[110:111]\n"
"v_cvt_pk_bf16_f32 v56, v48, v49\n"
"v_cvt_pk_bf16_f32 v57, v50, v51\n"
"v_cvt_pk_bf16_f32 v58, v52, v53\n"
"v_cvt_pk_bf16_f32 v59, v54, v55\n"
"buffer_store_dwordx2 v[56:57], v198, s[76:79], s96 offen\n"
"buffer_store_dwordx2 v[58:59], v198, s[76:79], s96 offen offset:1024\n"
"L_end:\n"
"s_endpgm\n"
"L_sp_pre:\n"
"s_call_b64 s[98:99], L_spinFL\n"
"s_branch L_r_pre\n"
"L_sp_p0:\n"
"s_call_b64 s[98:99], L_spinFL\n"
"s_branch L_r_p0\n"
"L_sp_a0:\n"
"s_call_b64 s[98:99], L_spinFL\n"
"s_branch L_r_a0\n"
"L_sp_a1:\n"
"s_call_b64 s[98:99], L_spinFL\n"
"s_branch L_r_a1\n"
"L_sp_a2:\n"
"s_call_b64 s[98:99], L_spinFL\n"
"s_branch L_r_a2\n"
"L_sp_a3:\n"
"s_call_b64 s[98:99], L_spinFL\n"
"s_branch L_r_a3\n"
"L_sp_last:\n"
"s_call_b64 s[98:99], L_spinFL\n"
"s_branch L_r_last\n"
"L_spinFL:\n"
"ds_read_addtid_b32 v252 offset:256\n"
"s_sub_u32 s93, s93, 1\n"
"s_cbranch_scc1 L_spinFLto\n"
"s_waitcnt lgkmcnt(0)\n"
"v_cmp_gt_u32 vcc, v251, v252\n"
"s_cbranch_vccnz L_spinFL\n"
"L_spinFLto:\n"
"s_max_i32 s93, s93, 0\n"
"s_waitcnt lgkmcnt(0)\n"
"s_setpc_b64 s[98:99]\n"
"L_sp_w0:\n"
"s_waitcnt vmcnt(0)\n"
"s_mov_b64 exec, 1\n"
"buffer_store_dwordx2 v[250:251], off, s[72:75], s65 sc1\n"
"s_mov_b64 exec, -1\n"
"L_sp_w:\n"
"ds_read_addtid_b32 v252 offset:256\n"
"s_sub_u32 s93, s93, 1\n"
"s_cbranch_scc1 L_sp_wto\n"
"s_waitcnt lgkmcnt(0)\n"
"v_cmp_gt_u32 vcc, 41, v252\n"
"s_cbranch_vccnz L_sp_w\n"
"L_sp_wto:\n"
"s_max_i32 s93, s93, 0\n"
"s_waitcnt lgkmcnt(0)\n"
"s_branch L_r_w\n"
"L_exit:\n"
"s_nop 15\n"
"s_waitcnt vmcnt(0) lgkmcnt(0)\n"
    : "+s"(ka), "+s"(g), "+s"(wid)
    : "s"((unsigned)(size_t)(__attribute__((address_space(3))) char*)lds)
    : "v0","v1","v2","v3","v4","v5","v6","v7","v8","v9","v10","v11","v12","v13","v14","v15","v16","v17","v18","v19","v20","v21","v22","v23","v24","v25","v26","v27","v28","v29","v30","v31","v32","v33","v34","v35","v36","v37","v38","v39","v40","v41","v42","v43","v44","v45","v46","v47","v48","v49","v50","v51","v52","v53","v54","v55","v56","v57","v58","v59","v60","v61","v62","v63","v64","v65","v66","v67","v68","v69","v70","v71","v72","v73","v74","v75","v76","v77","v78","v79","v80","v81","v82","v83","v84","v85","v86","v87","v88","v89","v90","v91","v92","v93","v94","v95","v96","v97","v98","v99","v100","v101","v102","v103","v104","v105","v106","v107","v108","v109","v110","v111","v112","v113","v114","v115","v116","v117","v118","v119","v120","v121","v122","v123","v124","v125","v126","v127","v128","v129","v130","v131","v132","v133","v134","v135","v136","v137","v138","v139","v140","v141","v142","v143","v144","v145","v146","v147","v148","v149","v150","v151","v152","v153","v154","v155","v156","v157","v158","v159","v160","v161","v162","v163","v164","v165","v166","v167","v168","v169","v170","v171","v172","v173","v174","v175","v176","v177","v178","v179","v180","v181","v182","v183","v184","v185","v186","v187","v188","v189","v190","v191","v192","v193","v194","v195","v196","v197","v198","v199","v200","v201","v202","v203","v204","v205","v206","v207","v208","v209","v210","v211","v212","v213","v214","v215","v216","v217","v218","v219","v220","v221","v222","v223","v224","v225","v226","v227","v228","v229","v230","v231","v232","v233","v234","v235","v236","v237","v238","v239","v240","v241","v242","v243","v244","v245","v246","v247","v248","v249","v250","v251","v252","v253","v254","v255","s36","s37","s38","s39","s40","s41","s42","s43","s44","s45","s46","s47","s48","s49","s50","s51","s52","s53","s54","s55","s56","s57","s58","s59","s60","s61","s62","s63","s64","s65","s66","s67","s68","s69","s70","s71","s72","s73","s74","s75","s76","s77","s78","s79","s80","s81","s82","s83","s84","s85","s86","s87","s88","s89","s90","s91","s92","s93","s94","s95","s96","s97","s98","s99","vcc","scc","memory");
  unsigned lane;
  asm volatile("v_mbcnt_lo_u32_b32 %0, -1, 0\n v_mbcnt_hi_u32_b32 %0, -1, %0" : "=v"(lane));
  const unsigned slot = g >> 3, b = (g & 1) * 4 + (slot & 3), h = ((g >> 1) & 3) * 8 + (slot >> 2);
  const u16 GL* x = (const u16 GL*)ka[0];
  const u16 GL* wq = (const u16 GL*)ka[3];
  const u16 GL* K = (const u16 GL*)ka[1] + (size_t)(b * 32 + h) * 131072;
  const u16 GL* V = (const u16 GL*)ka[2] + (size_t)(b * 32 + h) * 131072;
  char GL* O = (char GL*)ka[6] + 262144 + (size_t)((4 * b + (wid & 3)) * 32 + h) * 12288;
#pragma unroll 1
  for (int r = 0; r < 48; r++) {
    const u16 GL* xr = x + (size_t)(b * 192 + (wid & 3) * 48 + r) * 512;
    float q0 = 0.f, q1 = 0.f;
#pragma unroll 4
    for (int k = 0; k < 512; k++) {
      float xv = blo(xr[k]);
      unsigned w = *(const unsigned GL*)(wq + (size_t)k * 4096 + h * 128 + 2 * lane);
      q0 += xv * blo(w);
      q1 += xv * bhi(w);
    }
    q0 = blo(f2b(q0));
    q1 = blo(f2b(q1));
    float s[16];
#pragma unroll
    for (int i = 0; i < 16; i++) s[i] = 0.f;
#pragma unroll 1
    for (int c = 0; c < 16; c++) {
      float qq[8];
#pragma unroll
      for (int j = 0; j < 4; j++) { qq[2 * j] = rl(q0, 4 * c + j); qq[2 * j + 1] = rl(q1, 4 * c + j); }
#pragma unroll
      for (int i = 0; i < 16; i++) {
        u32x4 kk = *(const u32x4 GL*)(K + (size_t)(64 * i + lane) * 128 + 8 * c);
#pragma unroll
        for (int j = 0; j < 4; j++) s[i] += qq[2 * j] * blo(kk[j]) + qq[2 * j + 1] * bhi(kk[j]);
      }
    }
    float m = s[0];
#pragma unroll
    for (int i = 1; i < 16; i++) m = fmaxf(m, s[i]);
#pragma unroll
    for (int d = 1; d < 64; d <<= 1) m = fmaxf(m, bperm(m, lane ^ d));
    float l = 0.f;
#pragma unroll
    for (int i = 0; i < 16; i++) { s[i] = exp2f((s[i] - m) * 0.12751743082f); l += s[i]; }
#pragma unroll
    for (int d = 1; d < 64; d <<= 1) l += bperm(l, lane ^ d);
    float o0 = 0.f, o1 = 0.f;
#pragma unroll
    for (int i = 0; i < 16; i++) {
#pragma unroll 4
      for (int k = 0; k < 64; k++) {
        float p = rl(s[i], k);
        unsigned vv = *(const unsigned GL*)(V + (size_t)(64 * i + k) * 128 + 2 * lane);
        o0 += p * blo(vv);
        o1 += p * bhi(vv);
      }
    }
    l = 1.f / l;
    const unsigned d = 2 * lane, dd = d & 31;
    const unsigned off = (((d >> 5) * 3 + (r >> 4)) * 64 + (r & 15) + 16 * ((dd & 15) >> 2)) * 16 + 2 * ((dd & 3) + 4 * (dd >> 4));
    __hip_atomic_store((unsigned GL*)(O + off), f2b(o0 * l) | (f2b(o1 * l) << 16), __ATOMIC_RELAXED, __HIP_MEMORY_SCOPE_AGENT);
  }
  asm volatile(
    "s_load_dwordx8 s[36:43], %0, 0x0\n"
    "s_load_dwordx4 s[44:47], %0, 0x20\n"
    "s_load_dwordx2 s[48:49], %0, 0x30\n"
    "s_mov_b32 s50, %1\n"
    "s_mov_b32 s51, %2\n"
    "s_mov_b32 s52, 1\n"
    "s_mov_b64 exec, -1\n"
    "s_waitcnt vmcnt(0) lgkmcnt(0)\n"
    "s_branch L_setup\n"
    :
    : "s"(ka), "s"(g), "s"(wid)
    : "v0","v1","v2","v3","v4","v5","v6","v7","v8","v9","v10","v11","v12","v13","v14","v15","v16","v17","v18","v19","v20","v21","v22","v23","v24","v25","v26","v27","v28","v29","v30","v31","v32","v33","v34","v35","v36","v37","v38","v39","v40","v41","v42","v43","v44","v45","v46","v47","v48","v49","v50","v51","v52","v53","v54","v55","v56","v57","v58","v59","v60","v61","v62","v63","v64","v65","v66","v67","v68","v69","v70","v71","v72","v73","v74","v75","v76","v77","v78","v79","v80","v81","v82","v83","v84","v85","v86","v87","v88","v89","v90","v91","v92","v93","v94","v95","v96","v97","v98","v99","v100","v101","v102","v103","v104","v105","v106","v107","v108","v109","v110","v111","v112","v113","v114","v115","v116","v117","v118","v119","v120","v121","v122","v123","v124","v125","v126","v127","v128","v129","v130","v131","v132","v133","v134","v135","v136","v137","v138","v139","v140","v141","v142","v143","v144","v145","v146","v147","v148","v149","v150","v151","v152","v153","v154","v155","v156","v157","v158","v159","v160","v161","v162","v163","v164","v165","v166","v167","v168","v169","v170","v171","v172","v173","v174","v175","v176","v177","v178","v179","v180","v181","v182","v183","v184","v185","v186","v187","v188","v189","v190","v191","v192","v193","v194","v195","v196","v197","v198","v199","v200","v201","v202","v203","v204","v205","v206","v207","v208","v209","v210","v211","v212","v213","v214","v215","v216","v217","v218","v219","v220","v221","v222","v223","v224","v225","v226","v227","v228","v229","v230","v231","v232","v233","v234","v235","v236","v237","v238","v239","v240","v241","v242","v243","v244","v245","v246","v247","v248","v249","v250","v251","v252","v253","v254","v255","s36","s37","s38","s39","s40","s41","s42","s43","s44","s45","s46","s47","s48","s49","s50","s51","s52","s53","s54","s55","s56","s57","s58","s59","s60","s61","s62","s63","s64","s65","s66","s67","s68","s69","s70","s71","s72","s73","s74","s75","s76","s77","s78","s79","s80","s81","s82","s83","s84","s85","s86","s87","s88","s89","s90","s91","s92","s93","s94","s95","s96","s97","s98","s99","vcc","scc","memory");
}

extern "C" void kernel_launch(void* const* d_in, const int* in_sizes, int n_in, void* d_out, int out_size, void* d_ws, size_t ws_size,
                              hipStream_t stream)
{
  fused<<<256, 512, 0, stream>>>((const u16*)d_in[0], (const u16*)d_in[1], (const u16*)d_in[2], (const u16*)d_in[3], (const u16*)d_in[4],
                                 (u16*)d_out, (char*)d_ws);
}
```
